```python
import torch

def _softmax(x, axis=-1):
    return torch.softmax(x, dim=axis)
def _gelu(x):
    return torch.nn.functional.gelu(x, approximate="tanh")
def _silu(x):
    return torch.nn.functional.silu(x)
def _sigmoid(x):
    return torch.sigmoid(x)
def _layernorm(x, axis=-1, eps=1e-5):
    m = x.mean(dim=axis, keepdim=True)
    v = x.var(dim=axis, keepdim=True, unbiased=False)
    return (x - m) / torch.sqrt(v + eps)
def _scatter_add(tgt, idx, src, axis=0):
    return tgt.clone().index_add_(axis, idx, src)
def _segment_sum(src, seg, n):
    out = torch.zeros((n,) + src.shape[1:], dtype=src.dtype)
    return out.index_add_(0, seg, src)
def _pad(x, pw):
    flat = tuple(v for pair in reversed(pw) for v in pair)
    return torch.nn.functional.pad(x, flat)

kernel_name = 'recurrent_scan__sliding_block_cached__geglu_ffn__decode'

def setup_inputs():

    _w = (lambda a: (lambda t: t.to(torch.bfloat16) if t.is_floating_point() else t)(torch.as_tensor(a)))
    def _A_setup():
        B, Sq, D = 4, 1, 576
        Hq, Hkv, Sk, Dh = 32, 16, 8192, 128
        assert Hq % Hkv == 0, (Hq, Hkv)
        return {
            "x": _w((lambda *s: torch.randn(*s, dtype=torch.bfloat16) * 0.05)(B, Sq, D)),
            "k_cache": _w((lambda *s: torch.randn(*s, dtype=torch.bfloat16) * 0.8)(B, Hkv, Sk, Dh)),
            "v_cache": _w((lambda *s: torch.randn(*s, dtype=torch.bfloat16) * 0.2)(B, Hkv, Sk, Dh)),
            "wq": _w((lambda *s: torch.randn(*s, dtype=torch.bfloat16) * 0.8)(D, Hq * Dh) * (D ** -0.5)),
            "wo": _w((lambda *s: torch.randn(*s, dtype=torch.bfloat16) * 0.2)(Hq * Dh, D) * ((Hq * Dh) ** -0.5)),
        }
    _A_in = _A_setup()

    def _B_setup():
        B, S, D, F = 4, 1, 576, 4608
        return {
            "x": _w((lambda *s: torch.randn(*s, dtype=torch.bfloat16) * 0.05)(B, S, D)),
            "w_gate": _w((lambda *s: torch.randn(*s, dtype=torch.bfloat16) * 0.1)(D, F)),
            "w_up": _w((lambda *s: torch.randn(*s, dtype=torch.bfloat16) * 0.1)(D, F)),
            "w_down": _w((lambda *s: torch.randn(*s, dtype=torch.bfloat16) * 0.1)(F, D)),
        }
    _B_in = _B_setup()

    return {
        "A_x": _A_in["x"],
        "A_k_cache": _A_in["k_cache"],
        "A_v_cache": _A_in["v_cache"],
        "A_wq": _A_in["wq"],
        "A_wo": _A_in["wo"],
        "B_x": _B_in["x"],
        "B_w_gate": _B_in["w_gate"],
        "B_w_up": _B_in["w_up"],
        "B_w_down": _B_in["w_down"],
    }

def reference(A_x, A_k_cache, A_v_cache, A_wq, A_wo, B_x, B_w_gate, B_w_up, B_w_down):
    def _A_ref(x, k_cache, v_cache, wq, wo):
        B, Sq, D = x.shape
        Hq, Dh = 32, 128
        Hkv, Sk = k_cache.shape[1], k_cache.shape[2]
        W = min(4096, max(256, Sk // 4))
        rep = Hq // Hkv
        q = (lambda x, *d: x.permute(*d))((x @ wq).reshape(B, Sq, Hq, Dh), 0, 2, 1, 3)
        k = (lambda x, n, axis: torch.repeat_interleave(x, n, dim=axis))(k_cache[:, :, Sk - W:, :], rep, axis=1)
        v = (lambda x, n, axis: torch.repeat_interleave(x, n, dim=axis))(v_cache[:, :, Sk - W:, :], rep, axis=1)
        scores = torch.einsum("bhqd,bhkd->bhqk", q, k) * (Dh ** -0.5)
        p = _softmax(scores, axis=-1)
        o = torch.einsum("bhqk,bhkd->bhqd", p, v)
        return (lambda x, *d: x.permute(*d))(o, 0, 2, 1, 3).reshape(B, Sq, Hq * Dh) @ wo

    def _B_ref(x, w_gate, w_up, w_down):
        return (_gelu(x @ w_gate) * (x @ w_up)) @ w_down

    x = A_x
    xn = _layernorm(x, axis=-1)
    s = _A_ref(xn, A_k_cache, A_v_cache, A_wq, A_wo)
    g = _silu(_B_ref(xn, B_w_gate, B_w_up, B_w_down))
    return x + s * g
```

```cpp
#include <hip/hip_runtime.h>
#include <hip/hip_bf16.h>
#include <stdint.h>

#define GAS __attribute__((address_space(1)))
typedef unsigned v4u __attribute__((ext_vector_type(4)));
typedef float f32x4 __attribute__((ext_vector_type(4)));
typedef GAS const v4u gcv4u;
typedef __attribute__((ext_vector_type(2))) __bf16 bf16x2;

constexpr int B = 4, D = 576, HQ = 32, HKV = 16, SK = 8192, DH = 128, WIN = 2048, F = 4608, QD = HQ * DH;

__device__ __forceinline__ float bf2f(unsigned short b) { return __uint_as_float((unsigned)b << 16); }
__device__ __forceinline__ float lo(unsigned u) { return __uint_as_float(u << 16); }
__device__ __forceinline__ float hi(unsigned u) { return __uint_as_float(u & 0xffff0000u); }
__device__ __forceinline__ float rbf(float f) { return __bfloat162float(__float2bfloat16(f)); }
__device__ __forceinline__ unsigned short f2bfbits(float f) { __hip_bfloat16 h = __float2bfloat16(f); return *reinterpret_cast<unsigned short*>(&h); }
__device__ __forceinline__ float dot2(unsigned k, unsigned q, float acc) {
  return __builtin_amdgcn_fdot2_f32_bf16(__builtin_bit_cast(bf16x2, k), __builtin_bit_cast(bf16x2, q), acc, false);
}
__device__ __forceinline__ v4u ldnt(gcv4u* p) { return __builtin_nontemporal_load(p); }
__device__ __forceinline__ float wsum(float v) { for (int m = 32; m >= 1; m >>= 1) v += __shfl_xor(v, m); return v; }

__device__ __forceinline__ void layernorm_lds(const unsigned short* x, float* xn) {
  const int w = threadIdx.x >> 6, lane = threadIdx.x & 63;
  if (w < 4) {
    float v[9]; float s = 0.f;
#pragma unroll
    for (int i = 0; i < 9; ++i) { v[i] = bf2f(x[w * D + lane + 64 * i]); s += v[i]; }
    s = wsum(s); const float mean = s * (1.f / D); float q = 0.f;
#pragma unroll
    for (int i = 0; i < 9; ++i) { const float d = v[i] - mean; q += d * d; }
    q = wsum(q);
    const float mb = rbf(mean), vb = rbf(q * (1.f / D));
    const float den = rbf(sqrtf(rbf(vb + 1e-5f)));
#pragma unroll
    for (int i = 0; i < 9; ++i) xn[w * D + lane + 64 * i] = rbf(rbf(v[i] - mb) / den);
  }
}

__device__ __forceinline__ void gemv64(const unsigned short* W, int ldw, int col0, const float* xn, float* red  ) {
  const int w = threadIdx.x >> 6, lane = threadIdx.x & 63, r = lane >> 3, c = lane & 7;
  float acc[4][8];
#pragma unroll
  for (int b = 0; b < 4; ++b)
#pragma unroll
    for (int j = 0; j < 8; ++j) acc[b][j] = 0.f;
  v4u a[18];
#pragma unroll
  for (int i = 0; i < 18; ++i) { const int row = (i * 4 + w) * 8 + r; a[i] = ldnt((gcv4u*)(W + (size_t)row * ldw + col0 + c * 8)); }
#pragma unroll
  for (int i = 0; i < 18; ++i) { const int row = (i * 4 + w) * 8 + r;
#pragma unroll
    for (int b = 0; b < 4; ++b) { const float xv = xn[b * D + row];
#pragma unroll
      for (int k = 0; k < 4; ++k) { acc[b][2 * k] = fmaf(xv, lo(a[i][k]), acc[b][2 * k]); acc[b][2 * k + 1] = fmaf(xv, hi(a[i][k]), acc[b][2 * k + 1]); } } }
#pragma unroll
  for (int b = 0; b < 4; ++b)
#pragma unroll
    for (int j = 0; j < 8; ++j) { float v = acc[b][j]; v += __shfl_xor(v, 8); v += __shfl_xor(v, 16); v += __shfl_xor(v, 32); if (r == 0) red[(w * 4 + b) * 64 + c * 8 + j] = v; }
}

__global__ void __launch_bounds__(256) k_prep(const unsigned short* x, const unsigned short* wq, const unsigned short* wg, const unsigned short* wu, const unsigned short* wd,
                                              unsigned short* q  , float* fpart  ) {
  __shared__ float xn[4 * D]; __shared__ float red[4 * 4 * 64]; __shared__ float red2[4 * 4 * 64]; __shared__ float h[4 * 64]; __shared__ float fr[4][4 * D];
  layernorm_lds(x, xn); __syncthreads();
  const int t = threadIdx.x;
  if (blockIdx.x < 64) {
    gemv64(wq, QD, blockIdx.x * 64, xn, red); __syncthreads();
    const int b = t >> 6, c = t & 63;
    const float v = red[(0 * 4 + b) * 64 + c] + red[(1 * 4 + b) * 64 + c] + red[(2 * 4 + b) * 64 + c] + red[(3 * 4 + b) * 64 + c];
    q[b * QD + blockIdx.x * 64 + c] = f2bfbits(v);
  } else {
    const int s = blockIdx.x - 64;
    gemv64(wg, F, s * 64, xn, red); gemv64(wu, F, s * 64, xn, red2); __syncthreads();
    { const int b = t >> 6, c = t & 63;
      const float g = rbf(red[(0 * 4 + b) * 64 + c] + red[(1 * 4 + b) * 64 + c] + red[(2 * 4 + b) * 64 + c] + red[(3 * 4 + b) * 64 + c]);
      const float u = rbf(red2[(0 * 4 + b) * 64 + c] + red2[(1 * 4 + b) * 64 + c] + red2[(2 * 4 + b) * 64 + c] + red2[(3 * 4 + b) * 64 + c]);
      const float inner = 0.7978845608028654f * (g + 0.044715f * g * g * g);
      const float ge = rbf(0.5f * g * (1.f + tanhf(inner)));
      h[b * 64 + c] = rbf(ge * u); }
    __syncthreads();
    const int w = t >> 6, lane = t & 63;
    float acc[4][8], acc2[4][8];
#pragma unroll
    for (int b = 0; b < 4; ++b)
#pragma unroll
      for (int j = 0; j < 8; ++j) { acc[b][j] = 0.f; acc2[b][j] = 0.f; }
#pragma unroll 4
    for (int i = 0; i < 16; ++i) { const int row = w * 16 + i;
      const v4u a = ldnt((gcv4u*)(wd + (size_t)(s * 64 + row) * D + lane * 8));
      v4u a2 = {0, 0, 0, 0}; if (lane < 8) a2 = ldnt((gcv4u*)(wd + (size_t)(s * 64 + row) * D + 512 + lane * 8));
#pragma unroll
      for (int b = 0; b < 4; ++b) { const float hv = h[b * 64 + row];
#pragma unroll
        for (int k = 0; k < 4; ++k) { acc[b][2 * k] = fmaf(hv, lo(a[k]), acc[b][2 * k]); acc[b][2 * k + 1] = fmaf(hv, hi(a[k]), acc[b][2 * k + 1]);
          acc2[b][2 * k] = fmaf(hv, lo(a2[k]), acc2[b][2 * k]); acc2[b][2 * k + 1] = fmaf(hv, hi(a2[k]), acc2[b][2 * k + 1]); } } }
#pragma unroll
    for (int b = 0; b < 4; ++b)
#pragma unroll
      for (int j = 0; j < 8; ++j) { fr[w][b * D + lane * 8 + j] = acc[b][j]; if (lane < 8) fr[w][b * D + 512 + lane * 8 + j] = acc2[b][j]; }
    __syncthreads();
    for (int i = t; i < 4 * D; i += 256) fpart[(size_t)s * 4 * D + i] = fr[0][i] + fr[1][i] + fr[2][i] + fr[3][i];
  }
}

__global__ void __launch_bounds__(256) k_attn(const unsigned short* K, const unsigned short* V, const unsigned short* q, float* part  ) {
  __shared__ float sc[2][512]; __shared__ float wm[4][2]; __shared__ float ob[4][2][128]; __shared__ float wl[4][2];
  const int wg = blockIdx.x, pair = (wg & 7) * 8 + (wg >> 5), quarter = (wg >> 3) & 3, b = pair >> 4, kvh = pair & 15;
  const int t = threadIdx.x, w = t >> 6, lane = t & 63, r = lane >> 4, c = lane & 15;
  gcv4u* kb = (gcv4u*)(K + ((size_t)pair * SK + (SK - WIN) + quarter * 512) * DH) + lane;
  gcv4u* vb = (gcv4u*)(V + ((size_t)pair * SK + (SK - WIN) + quarter * 512) * DH) + lane;
  v4u ka[32];
#pragma unroll
  for (int i = 0; i < 32; ++i) ka[i] = ldnt(kb + (i * 4 + w) * 64);
  const v4u q0 = *(gcv4u*)(q + b * QD + (kvh * 2) * DH + c * 8), q1 = *(gcv4u*)(q + b * QD + (kvh * 2 + 1) * DH + c * 8);
  __builtin_amdgcn_sched_barrier(0);
  const float C2 = 0.08838834764831845f * 1.4426950408889634f;
  float m0 = -INFINITY, m1 = -INFINITY;
  v4u va[32];
#pragma unroll
  for (int i = 0; i < 32; ++i) {
    float s0 = 0.f, s1 = 0.f;
#pragma unroll
    for (int k = 0; k < 4; ++k) { s0 = dot2(ka[i][k], q0[k], s0); s1 = dot2(ka[i][k], q1[k], s1); }
    va[i] = ldnt(vb + (i * 4 + w) * 64);
    s0 += __shfl_xor(s0, 1); s1 += __shfl_xor(s1, 1); s0 += __shfl_xor(s0, 2); s1 += __shfl_xor(s1, 2);
    s0 += __shfl_xor(s0, 4); s1 += __shfl_xor(s1, 4); s0 += __shfl_xor(s0, 8); s1 += __shfl_xor(s1, 8);
    s0 *= C2; s1 *= C2; m0 = fmaxf(m0, s0); m1 = fmaxf(m1, s1);
    if (c == 0) { sc[0][(i * 4 + w) * 4 + r] = s0; sc[1][(i * 4 + w) * 4 + r] = s1; }
  }
  m0 = fmaxf(m0, __shfl_xor(m0, 16)); m0 = fmaxf(m0, __shfl_xor(m0, 32)); m1 = fmaxf(m1, __shfl_xor(m1, 16)); m1 = fmaxf(m1, __shfl_xor(m1, 32));
  float acc0[8], acc1[8], l0 = 0.f, l1 = 0.f;
#pragma unroll
  for (int j = 0; j < 8; ++j) { acc0[j] = 0.f; acc1[j] = 0.f; }
#pragma unroll
  for (int i = 0; i < 32; ++i) {
    const float p0 = exp2f(sc[0][(i * 4 + w) * 4 + r] - m0), p1 = exp2f(sc[1][(i * 4 + w) * 4 + r] - m1);
    l0 += p0; l1 += p1;
#pragma unroll
    for (int k = 0; k < 4; ++k) { const float a = lo(va[i][k]), bb = hi(va[i][k]);
      acc0[2 * k] = fmaf(p0, a, acc0[2 * k]); acc0[2 * k + 1] = fmaf(p0, bb, acc0[2 * k + 1]); acc1[2 * k] = fmaf(p1, a, acc1[2 * k]); acc1[2 * k + 1] = fmaf(p1, bb, acc1[2 * k + 1]); }
  }
  l0 += __shfl_xor(l0, 16); l0 += __shfl_xor(l0, 32); l1 += __shfl_xor(l1, 16); l1 += __shfl_xor(l1, 32);
#pragma unroll
  for (int j = 0; j < 8; ++j) { acc0[j] += __shfl_xor(acc0[j], 16); acc0[j] += __shfl_xor(acc0[j], 32); acc1[j] += __shfl_xor(acc1[j], 16); acc1[j] += __shfl_xor(acc1[j], 32); }
  if (r == 0) {
#pragma unroll
    for (int j = 0; j < 8; ++j) { ob[w][0][c * 8 + j] = acc0[j]; ob[w][1][c * 8 + j] = acc1[j]; }
    if (c == 0) { wm[w][0] = m0; wm[w][1] = m1; wl[w][0] = l0; wl[w][1] = l1; }
  }
  __syncthreads();
  { const int hh = t >> 7, d = t & 127;
    const float M = fmaxf(fmaxf(wm[0][hh], wm[1][hh]), fmaxf(wm[2][hh], wm[3][hh]));
    float o = 0.f, L = 0.f;
#pragma unroll
    for (int ww = 0; ww < 4; ++ww) { const float e = exp2f(wm[ww][hh] - M); o += e * ob[ww][hh][d]; L += e * wl[ww][hh]; }
    float* p = part + ((size_t)(pair * 4 + quarter) * 2 + hh) * 130;
    p[2 + d] = o; if (d == 0) { p[0] = M; p[1] = L; } }
}

__global__ void __launch_bounds__(1024) k_final(const unsigned short* x, const unsigned short* wo, const float* part, const float* fpart, unsigned short* out) {
  __shared__ float o[4 * QD]; __shared__ float red[16][4][64];
  const int t = threadIdx.x, w = t >> 6, lane = t & 63;
  for (int i = t; i < 4 * QD; i += 1024) { const int b = i >> 12, hq = (i >> 7) & 31, d = i & 127, pair = b * 16 + (hq >> 1), hh = hq & 1;
    const float* p = part + ((size_t)(pair * 4) * 2 + hh) * 130;
    const float M = fmaxf(fmaxf(p[0], p[260]), fmaxf(p[520], p[780]));
    float oo = 0.f, L = 0.f;
#pragma unroll
    for (int s = 0; s < 4; ++s) { const float e = exp2f(p[260 * s] - M); oo += e * p[260 * s + 2 + d]; L += e * p[260 * s + 1]; }
    o[i] = rbf(oo / L); }
  __syncthreads();
  const int r = lane >> 3, c = lane & 7, col0 = blockIdx.x * 64;
  float acc[4][8];
#pragma unroll
  for (int b = 0; b < 4; ++b)
#pragma unroll
    for (int j = 0; j < 8; ++j) acc[b][j] = 0.f;
#pragma unroll 8
  for (int i = 0; i < 32; ++i) { const int row = (i * 16 + w) * 8 + r; const v4u a = ldnt((gcv4u*)(wo + (size_t)row * D + col0 + c * 8));
#pragma unroll
    for (int b = 0; b < 4; ++b) { const float ov = o[b * QD + row];
#pragma unroll
      for (int k = 0; k < 4; ++k) { acc[b][2 * k] = fmaf(ov, lo(a[k]), acc[b][2 * k]); acc[b][2 * k + 1] = fmaf(ov, hi(a[k]), acc[b][2 * k + 1]); } } }
#pragma unroll
  for (int b = 0; b < 4; ++b)
#pragma unroll
    for (int j = 0; j < 8; ++j) { float v = acc[b][j]; v += __shfl_xor(v, 8); v += __shfl_xor(v, 16); v += __shfl_xor(v, 32); if (r == 0) red[w][b][c * 8 + j] = v; }
  __syncthreads();
  if (t < 256) { const int b = t >> 6, cc = t & 63; float s = 0.f;
    for (int ww = 0; ww < 16; ++ww) s += red[ww][b][cc];
    float f = 0.f;
    for (int k = 0; k < 72; ++k) f += fpart[(size_t)k * 4 * D + b * D + col0 + cc];
    s = rbf(s); f = rbf(f);
    const float g = rbf(f / (1.f + __expf(-f)));
    out[b * D + col0 + cc] = f2bfbits(bf2f(x[b * D + col0 + cc]) + rbf(s * g)); }
}

extern "C" void kernel_launch(void* const* d_in, const int* in_sizes, int n_in, void* d_out, int out_size, void* d_ws, size_t ws_size, hipStream_t stream) {
  const unsigned short* x = (const unsigned short*)d_in[0];
  const unsigned short* K = (const unsigned short*)d_in[1];
  const unsigned short* V = (const unsigned short*)d_in[2];
  const unsigned short* wq = (const unsigned short*)d_in[3];
  const unsigned short* wo = (const unsigned short*)d_in[4];
  const unsigned short* wg = (const unsigned short*)d_in[6];
  const unsigned short* wu = (const unsigned short*)d_in[7];
  const unsigned short* wd = (const unsigned short*)d_in[8];
  char* ws = (char*)d_ws;
  unsigned short* q = (unsigned short*)ws; float* part = (float*)(ws + 65536); float* fpart = (float*)(ws + (1 << 20));
  k_prep<<<136, 256, 0, stream>>>(x, wq, wg, wu, wd, q, fpart);
  k_attn<<<256, 256, 0, stream>>>(K, V, q, part);
  k_final<<<9, 1024, 0, stream>>>(x, wo, part, fpart, (unsigned short*)d_out);
}
```

```cpp
#include <hip/hip_runtime.h>
#include <hip/hip_bf16.h>
#include <stdint.h>

#define LAS __attribute__((address_space(3)))
typedef unsigned v4u __attribute__((ext_vector_type(4)));
typedef unsigned v2u __attribute__((ext_vector_type(2)));
typedef float f32x4 __attribute__((ext_vector_type(4)));
typedef short s16x4 __attribute__((ext_vector_type(4)));
typedef __bf16 bf16x2 __attribute__((ext_vector_type(2)));
typedef __bf16 bf16x8 __attribute__((ext_vector_type(8)));
typedef unsigned short u16;
typedef __amdgpu_buffer_rsrc_t rsrc_t;

namespace {
constexpr int DM = 576, SK = 8192, DH = 128, WIN = 2048, FF = 4608, QD = 4096;
constexpr int AUX_NT = 2, AUX_SC1 = 16;
#define KV_AUX 2
#define GATHER_AUX 16
#define WINDOW 8
#define Q_DELAY 420
#define H_DELAY 480
#define POLL_SLEEP 4
#define EARLY_WIN 48
constexpr int WND = WINDOW;
constexpr unsigned SPIN_CAP = 30000;

constexpr int WS_EPOCH = 0;
constexpr int WS_EARLY = 4096;
constexpr int WS_PART = WS_EARLY + 208 * 1024;
constexpr int WS_STAT = WS_PART + 256 * 1024;
constexpr int WS_FP = WS_STAT + 4096;
constexpr int WS_SP = WS_FP + 81 * 2048;
constexpr int WS_END = WS_SP + 144 * 2048;
constexpr int WS_STAMP = 8 << 20;

constexpr int L_WT = 0;
constexpr int L_LATE = 73728;
constexpr int L_AX = 106496, AX_STR = 1216;
constexpr int L_AH = L_AX + 4 * AX_STR, AH_STR = 1600;
constexpr int L_AO = L_AH + 4 * AH_STR, AO_STR = 576;
constexpr int L_Q = L_AO + 4 * AO_STR;
constexpr int L_PW = L_Q + 512;
constexpr int L_PS = L_PW + 4096;
constexpr int L_EP = L_PS + 64;
constexpr int L_FLAG = L_EP + 128;
constexpr int F_TAG = L_FLAG, F_Q = L_FLAG + 4, F_VISS = L_FLAG + 8, F_XN = L_FLAG + 12, F_PART = L_FLAG + 16, F_EARLY = L_FLAG + 20;
constexpr int L_BYTES = L_FLAG + 64;
static_assert(L_BYTES <= 160 * 1024 && L_BYTES > 80 * 1024, "one workgroup per CU");

#define ST(k) do { } while (0)
#define ST_DECL
#define ST_FLUSH() do { } while (0)

__device__ __forceinline__ rsrc_t mk_rsrc(const void* p, int bytes) { return __builtin_amdgcn_make_buffer_rsrc(const_cast<void*>(p), 0, bytes, 0x00020000); }
__device__ __forceinline__ float blo(unsigned u) { return __uint_as_float(u << 16); }
__device__ __forceinline__ float bhi(unsigned u) { return __uint_as_float(u & 0xffff0000u); }
__device__ __forceinline__ unsigned pk2(float lo, float hi) {
  bf16x2 r; r[0] = (__bf16)lo; r[1] = (__bf16)hi; return __builtin_bit_cast(unsigned, r);
}
__device__ __forceinline__ float rbf(float f) { return (float)(__bf16)f; }
__device__ __forceinline__ float dot2(unsigned k, unsigned q, float acc) {
  return __builtin_amdgcn_fdot2_f32_bf16(__builtin_bit_cast(bf16x2, k), __builtin_bit_cast(bf16x2, q), acc, false);
}
template <int CTRL> __device__ __forceinline__ float dpp(float x) {
  return __builtin_bit_cast(float, __builtin_amdgcn_mov_dpp(__builtin_bit_cast(int, x), CTRL, 0xf, 0xf, true));
}
template <int CTRL> __device__ __forceinline__ unsigned dppu(unsigned x) { return (unsigned)__builtin_amdgcn_mov_dpp((int)x, CTRL, 0xf, 0xf, true); }
constexpr int XOR1 = 0xB1, XOR2 = 0x4E, XOR7 = 0x141, ROR8 = 0x128, ROR4 = 0x124, ROR2 = 0x122, ROR1 = 0x121, BCAST = 0x150;
__device__ __forceinline__ float xrow_max(float x) {
  auto s = __builtin_amdgcn_permlane16_swap(__float_as_uint(x), __float_as_uint(x), false, false);
  x = fmaxf(__uint_as_float(s[0]), __uint_as_float(s[1]));
  auto t = __builtin_amdgcn_permlane32_swap(__float_as_uint(x), __float_as_uint(x), false, false);
  return fmaxf(__uint_as_float(t[0]), __uint_as_float(t[1]));
}
__device__ __forceinline__ float xrow_sum(float x) {
  auto s = __builtin_amdgcn_permlane16_swap(__float_as_uint(x), __float_as_uint(x), false, false);
  x = __uint_as_float(s[0]) + __uint_as_float(s[1]);
  auto t = __builtin_amdgcn_permlane32_swap(__float_as_uint(x), __float_as_uint(x), false, false);
  return __uint_as_float(t[0]) + __uint_as_float(t[1]);
}
__device__ __forceinline__ float row_sum(float x) { x += dpp<ROR8>(x); x += dpp<ROR4>(x); x += dpp<ROR2>(x); x += dpp<ROR1>(x); return x; }
__device__ __forceinline__ float row_max(float x) { x = fmaxf(x, dpp<ROR8>(x)); x = fmaxf(x, dpp<ROR4>(x)); x = fmaxf(x, dpp<ROR2>(x)); x = fmaxf(x, dpp<ROR1>(x)); return x; }
__device__ __forceinline__ f32x4 mfma(v4u a, v4u b, f32x4 c) {
  return __builtin_amdgcn_mfma_f32_16x16x32_bf16(__builtin_bit_cast(bf16x8, a), __builtin_bit_cast(bf16x8, b), c, 0, 0, 0);
}
#define TAGOK(v) ((((v).y ^ tag) | ((v).w ^ tag)) == 0u)
#define TOUCH(x) asm volatile("" ::"v"(x))
#define PACE() asm volatile("s_cmp_lg_u32 %0, 0\n\ts_cbranch_scc1 .Lpace%=\n\ts_waitcnt vmcnt(%1)\n.Lpace%=:" ::"s"(unpaced), "i"(WND - 1) : "scc")
#define SCHED() __builtin_amdgcn_sched_barrier(0)

typedef LAS unsigned char* lds_t;
__device__ __forceinline__ v2u tr_read(lds_t p) { return __builtin_bit_cast(v2u, __builtin_amdgcn_ds_read_tr16_b64_v4i16((LAS s16x4*)p)); }
__device__ __forceinline__ unsigned lds_ld(lds_t p) { return __hip_atomic_load((LAS unsigned*)p, __ATOMIC_RELAXED, __HIP_MEMORY_SCOPE_WORKGROUP); }
__device__ __forceinline__ void lds_st(lds_t p, unsigned v) { __hip_atomic_store((LAS unsigned*)p, v, __ATOMIC_RELAXED, __HIP_MEMORY_SCOPE_WORKGROUP); }
__device__ __forceinline__ void lds_wait_ge(lds_t p, unsigned want) {
  while ((unsigned)__builtin_amdgcn_readfirstlane(lds_ld(p)) < want) __builtin_amdgcn_s_sleep(1);
  asm volatile("" ::: "memory");
}
__device__ __forceinline__ void dma_8pieces(rsrc_t r, int voff, int& soff, int step, unsigned ldsoff) {
#define DMA_NEXT "s_add_u32 m0, m0, 0x400\n\ts_add_u32 %0, %0, %4\n\tbuffer_load_dwordx4 %2, %3, %0 offen nt lds\n\t"
  asm volatile("s_mov_b32 m0, %1\n\ts_nop 0\n\tbuffer_load_dwordx4 %2, %3, %0 offen nt lds\n\t" DMA_NEXT DMA_NEXT DMA_NEXT DMA_NEXT DMA_NEXT DMA_NEXT DMA_NEXT
               "s_add_u32 %0, %0, %4"
               : "+s"(soff) : "s"(ldsoff), "v"(voff), "s"(r), "s"(step) : "memory", "scc");
#undef DMA_NEXT
}
__device__ __forceinline__ int aperm(int k) { const int g = (k >> 2) & 7; return (k >> 5) * 64 + (g & 3) * 16 + (g >> 2) * 8; }

__device__ __forceinline__ void gemv_2steps(lds_t wt, const int (&tro)[4], lds_t ap, f32x4 (&acc)[4]) {
  v2u lo[2][4], hi[2][4]; v4u b[2];
#pragma unroll
  for (int s = 0; s < 2; ++s) {
    b[s] = *(LAS v4u*)(ap + s * 64);
#pragma unroll
    for (int t = 0; t < 4; ++t) { lo[s][t] = tr_read(wt + tro[t] + s * 4096); hi[s][t] = tr_read(wt + tro[t] + s * 4096 + 2048); }
  }
#pragma unroll
  for (int s = 0; s < 2; ++s)
#pragma unroll
    for (int t = 0; t < 4; ++t) acc[t] = mfma(v4u{lo[s][t].x, lo[s][t].y, hi[s][t].x, hi[s][t].y}, b[s], acc[t]);
}
__device__ __forceinline__ f32x4 sel2(bool c, f32x4 a, f32x4 b) { return f32x4{c ? b[0] : a[0], c ? b[1] : a[1], c ? b[2] : a[2], c ? b[3] : a[3]}; }

template <int N> __device__ __forceinline__ void wait_vm() { asm volatile("s_waitcnt vmcnt(%0)" ::"i"(N) : "memory"); }

__global__ void __launch_bounds__(384, 2)
fused_decode(const u16* __restrict__ x, const u16* __restrict__ Kc, const u16* __restrict__ Vc, const u16* __restrict__ wq, const u16* __restrict__ wo,
             const u16* __restrict__ wgate, const u16* __restrict__ wup, const u16* __restrict__ wdn, u16* __restrict__ out, unsigned char* __restrict__ ws) {
  __shared__ __attribute__((aligned(16))) unsigned char smem[L_BYTES];
  lds_t L = (lds_t)smem;
  const int lane = threadIdx.x & 63, w = __builtin_amdgcn_readfirstlane(threadIdx.x >> 6);
  const int wg = blockIdx.x, xcd = wg & 7, idx = wg >> 3;
  const int kvh = xcd * 2 + (idx >> 4), gi = idx & 15, bat = gi >> 2, quarter = gi & 3;
  const bool isA = gi < 9;
  int eu = -1, fcg = -1, ftile = 0;
  bool kfirst = false;
  {
    int n = -1;
    if (isA) n = kvh * 9 + gi;
    else {
      const int o = (idx >> 4) * 7 + (gi - 9);
      int hx = -1;
      if (xcd & 1) {
        if (o < 7) hx = 53 + 7 * (xcd >> 1) + o;
        else { const int j = 7 * (xcd >> 1) + (o - 7); if (j < 26) eu = 8 * j + 3; else n = 154 + j; }
      } else if (o < 13 || xcd == 6) hx = 13 * (xcd >> 1) + o;
      else kfirst = true;
      if (hx >= 45) { const int j = hx - 45; n = 144 + j; fcg = j >> 2; ftile = 5 + (j & 3); }
      else if (hx >= 0) { fcg = hx / 5; ftile = hx - 5 * fcg; }
    }
    if (n >= 0) { const int q7 = n / 7, r7 = n - 7 * q7; eu = 8 * q7 + r7 + (r7 >= 3); }
  }
  const bool isB = fcg >= 0 && ftile < 5, isC = fcg >= 0 && ftile >= 5;
  const int frow0 = ftile < 5 ? ftile * 768 : 3840 + (ftile - 5) * 192;
  const rsrc_t rws = mk_rsrc(ws, WS_END);
  ST_DECL;
  ST(0);

  if (w == 4) {
    __builtin_amdgcn_s_setprio(3);
    const unsigned long long t0 = __builtin_amdgcn_s_memrealtime();
    const int prow = lane >> 3, pch = (lane & 7) ^ (((prow >> 1) & 3) << 1);
    {
      const u16* base; int stride, n;
      if (eu >= 136) { base = wup + (eu - 136) * 64; stride = FF * 2; n = 72; }
      else if (eu >= 64) { base = wgate + (eu - 64) * 64; stride = FF * 2; n = 72; }
      else if (eu >= 0) { base = wq + eu * 64; stride = QD * 2; n = 72; }
      else { base = wdn + (size_t)frow0 * DM + (fcg < 0 ? 0 : fcg) * 64; stride = DM * 2; n = isB ? 64 : 0; }
      const rsrc_t r = mk_rsrc(base, n > 0 ? (n * 8 - 1) * stride + 128 : 0);
      const int voff = prow * stride + pch * 16;
      int soff = 0;
      if (n > 0) dma_8pieces(r, voff, soff, 8 * stride, (unsigned)(size_t)(L + L_WT));
      __builtin_amdgcn_s_barrier();
#pragma unroll 1
      for (int d = 8; d < n; d += 8) {
        wait_vm<EARLY_WIN>();
        dma_8pieces(r, voff, soff, 8 * stride, (unsigned)(size_t)(L + L_WT) + d * 1024);
      }
    }
    if (lane == 0) lds_st(L + F_EARLY, 1u);
    ST(1);
    lds_wait_ge(L + F_XN, 1);
    const unsigned tag = lds_ld(L + F_TAG);
    int tro[4];
    {
      const int g = lane >> 4, q = (lane >> 2) & 3, p = lane & 3, sw = ((2 * g + (q >> 1)) & 3) << 1;
#pragma unroll
      for (int t = 0; t < 4; ++t) tro[t] = (4 * g + q) * 128 + (((2 * t + (p >> 1)) ^ sw) << 4) + (p & 1) * 8;
    }
    const int g4 = lane >> 4, c = lane & 15;
    ST(2);
    if (eu >= 0) {
      f32x4 acc[4] = {};
      lds_t ap = L + L_AX + (lane & 3) * AX_STR + g4 * 16;
#define EARLY_STEP(j) wait_vm<(64 - 8 * (j)) < 56 ? (64 - 8 * (j)) : 56>(); gemv_2steps(L + L_WT + (j) * 8192, tro, ap + (j) * 128, acc);
      EARLY_STEP(0) EARLY_STEP(1) EARLY_STEP(2) EARLY_STEP(3) EARLY_STEP(4) EARLY_STEP(5) EARLY_STEP(6) EARLY_STEP(7) EARLY_STEP(8)
#undef EARLY_STEP
      const f32x4 y = sel2(c & 8, sel2(c & 4, acc[0], acc[1]), sel2(c & 4, acc[2], acc[3]));
      __builtin_amdgcn_raw_buffer_store_b128(v4u{pk2(y[0], y[1]), tag, pk2(y[2], y[3]), tag}, rws, WS_EARLY + eu * 1024 + (c & 3) * 256 + ((c >> 2) * 4 + g4) * 16, 0, AUX_SC1);
    }
    ST(3);
    f32x4 facc[4] = {};
    lds_t hp = L + L_AH + (lane & 3) * AH_STR + g4 * 16;
    if (fcg >= 0) {
      while (__builtin_amdgcn_s_memrealtime() - t0 < H_DELAY) __builtin_amdgcn_s_sleep(2);
      const int ncg = isB ? 12 : 3;
      const int off = WS_EARLY + (64 + (frow0 >> 6)) * 1024 + lane * 16;
      v4u g[12], u[12];
      unsigned miss = (1u << ncg) - 1u;
      for (unsigned spin = 0; spin < SPIN_CAP; ++spin) {
        asm volatile("" ::: "memory");
#pragma unroll
        for (int k = 0; k < 12; k += 3)
          if (miss & (7u << k)) {
#pragma unroll
            for (int e = k; e < k + 3; ++e) { g[e] = __builtin_amdgcn_raw_buffer_load_b128(rws, off, e * 1024, GATHER_AUX); u[e] = __builtin_amdgcn_raw_buffer_load_b128(rws, off, (72 + e) * 1024, GATHER_AUX); }
          }
#pragma unroll
        for (int k = 0; k < 12; k += 3)
          if (miss & (7u << k)) {
            const bool ok = TAGOK(g[k]) & TAGOK(u[k]) & TAGOK(g[k + 1]) & TAGOK(u[k + 1]) & TAGOK(g[k + 2]) & TAGOK(u[k + 2]);
            if (__all(ok)) {
              miss &= ~(7u << k);
#pragma unroll
              for (int e = k; e < k + 3; ++e) {
                const float gv[4] = {blo(g[e].x), bhi(g[e].x), blo(g[e].z), bhi(g[e].z)}, uv[4] = {blo(u[e].x), bhi(u[e].x), blo(u[e].z), bhi(u[e].z)};
                float hv[4];
#pragma unroll
                for (int i = 0; i < 4; ++i) {
                  const float a = gv[i], inner = 0.7978845608028654f * fmaf(0.044715f * a * a, a, a);
                  hv[i] = rbf(a * __builtin_amdgcn_rcpf(1.f + __builtin_amdgcn_exp2f(-2.8853900817779268f * inner))) * uv[i];
                }
                *(LAS v2u*)(L + L_AH + g4 * AH_STR + aperm(e * 64 + c * 4)) = v2u{pk2(hv[0], hv[1]), pk2(hv[2], hv[3])};
              }
            }
          }
        if (!miss) break;
      }
      ST(8);
      if (isB) {
        wait_vm<0>();
#pragma unroll 1
        for (int j = 0; j < 8; ++j) gemv_2steps(L + L_WT + j * 8192, tro, hp + j * 128, facc);
        hp += 1024;
      }
      ST(9);
    }
    if (isA || fcg >= 0) {
      lds_wait_ge(L + F_VISS, 4);
      ST(4);
      const u16* base = isA ? wo + (size_t)kvh * 256 * DM + gi * 64 : wdn + (size_t)(frow0 + (isB ? 512 : 0)) * DM + fcg * 64;
      const int n = isC ? 24 : 32;
      const rsrc_t r = mk_rsrc(base, (n * 8 - 1) * (DM * 2) + 128);
      const int voff = prow * (DM * 2) + pch * 16;
      int soff = 0;
#pragma unroll 1
      for (int d = 0; d < n; d += 8) dma_8pieces(r, voff, soff, 8 * (DM * 2), (unsigned)(size_t)(L + L_LATE) + d * 1024);
      ST(5);
      wait_vm<0>();
      ST(6);
      if (isA) {
        __builtin_amdgcn_s_barrier();
      } else {
#pragma unroll 1
        for (int j = 0; j < n / 8; ++j) gemv_2steps(L + L_LATE + j * 8192, tro, hp + j * 128, facc);
        const int x4 = c >> 2;
        const f32x4 y0 = sel2(x4 & 2, facc[0], facc[1]), y1 = sel2(x4 & 2, facc[2], facc[3]);
        const bool u = x4 & 1;
        const int off = WS_FP + (fcg * 9 + ftile) * 2048 + (c & 3) * 512 + ((x4 >> 1) * 8 + 2 * g4 + (x4 & 1)) * 16;
        __builtin_amdgcn_raw_buffer_store_b128(v4u{__float_as_uint(u ? y0[2] : y0[0]), tag, __float_as_uint(u ? y0[3] : y0[1]), tag}, rws, off, 0, AUX_SC1);
        __builtin_amdgcn_raw_buffer_store_b128(v4u{__float_as_uint(u ? y1[2] : y1[0]), tag, __float_as_uint(u ? y1[3] : y1[1]), tag}, rws, off + 256, 0, AUX_SC1);
      }
    }
    ST(7);
    ST_FLUSH();
    return;
  }

  if (w == 5) {
    __builtin_amdgcn_s_setprio(3);
    const unsigned long long t0 = __builtin_amdgcn_s_memrealtime();
    const rsrc_t rx = mk_rsrc(x, 4 * DM * 2);
    const int r = lane >> 4, c = lane & 15;
    v4u xv[5];
#pragma unroll
    for (int i = 0; i < 5; ++i) xv[i] = __builtin_amdgcn_raw_buffer_load_b128(rx, (i < 4 || c < 8) ? r * (DM * 2) + (c + 16 * i) * 16 : 0x10000, 0, 0);
    const unsigned ep = __builtin_amdgcn_raw_buffer_load_b32(rws, WS_EPOCH + (lane & 31) * 4, 0, AUX_SC1);
    __builtin_amdgcn_s_barrier();
    {
      float s = 0.f;
#pragma unroll
      for (int i = 0; i < 5; ++i)
#pragma unroll
        for (int k = 0; k < 4; ++k) s += blo(xv[i][k]) + bhi(xv[i][k]);
      const float mean = row_sum(s) * (1.f / DM);
      float q = 0.f;
#pragma unroll
      for (int i = 0; i < 5; ++i)
#pragma unroll
        for (int k = 0; k < 4; ++k) {
          const float a = blo(xv[i][k]) - mean, b = bhi(xv[i][k]) - mean;
          const float d = fmaf(a, a, b * b);
          q += (i < 4 || c < 8) ? d : 0.f;
        }
      const float var = rbf(row_sum(q) * (1.f / DM)), mb = rbf(mean);
      const float den = rbf(sqrtf(rbf(var + 1e-5f)));
      const float inv = __builtin_amdgcn_rcpf(den);
#pragma unroll
      for (int i = 0; i < 5; ++i) {
        unsigned o[4];
#pragma unroll
        for (int k = 0; k < 4; ++k) {
          const unsigned d = pk2(blo(xv[i][k]) - mb, bhi(xv[i][k]) - mb);
          o[k] = pk2(blo(d) * inv, bhi(d) * inv);
        }
        if (i < 4 || c < 8) {
          const int k0 = (c + 16 * i) * 8;
          *(LAS v2u*)(L + L_AX + r * AX_STR + aperm(k0)) = v2u{o[0], o[1]};
          *(LAS v2u*)(L + L_AX + r * AX_STR + aperm(k0 + 4)) = v2u{o[2], o[3]};
        }
      }
    }
    unsigned tag = ep;
    tag += dppu<ROR8>(tag); tag += dppu<ROR4>(tag); tag += dppu<ROR2>(tag); tag += dppu<ROR1>(tag);
    tag = (unsigned)__builtin_amdgcn_readlane(tag, 0) + (unsigned)__builtin_amdgcn_readlane(tag, 16) + 0x9E3779B9u;
    if (lane < 32) lds_st(L + L_EP + lane * 4, ep);
    if (lane == 0) lds_st(L + F_TAG, tag);
    asm volatile("s_waitcnt lgkmcnt(0)" ::: "memory");
    if (lane == 0) lds_st(L + F_XN, 1u);
    ST(1);
    while (__builtin_amdgcn_s_memrealtime() - t0 < Q_DELAY) __builtin_amdgcn_s_sleep(2);
    ST(2);
    {
      const int off = WS_EARLY + (kvh * 4 + r) * 1024 + bat * 256 + c * 16;
      v4u g;
      for (unsigned spin = 0; spin < SPIN_CAP; ++spin) {
        asm volatile("" ::: "memory");
        g = __builtin_amdgcn_raw_buffer_load_b128(rws, off, 0, GATHER_AUX);
        if (__all(TAGOK(g))) break;
      }
      *(LAS v2u*)(L + L_Q + lane * 8) = v2u{g.x, g.z};
      asm volatile("s_waitcnt lgkmcnt(0)" ::: "memory");
      if (lane == 0) lds_st(L + F_Q, 1u);
    }
    ST(3);
    ST(4);
    ST_FLUSH();
    return;
  }

  const int r = lane >> 4, c = lane & 15;
  const size_t kvbase = ((size_t)(bat * 16 + kvh) * SK + (SK - WIN) + quarter * 512) * DH;
  const rsrc_t rk = mk_rsrc(Kc + kvbase, 131072), rv = mk_rsrc(Vc + kvbase, 131072);
  const int vo = lane * 16 + w * 1024;
  unsigned xres = 0;
  const int ob = kvh >> 2, ocol = gi * 64 + (kvh & 3) * 16 + (lane & 7) * 2;
  if (w == 0) {
    if (lane < 8) lds_st(L + L_FLAG + lane * 4, 0u);
    asm volatile("s_waitcnt lgkmcnt(0)" ::: "memory");
    if (isA) xres = *(const unsigned*)(x + ob * DM + ocol);
  }
  __builtin_amdgcn_s_barrier();
  asm volatile("" ::: "memory");
  if (!kfirst) lds_wait_ge(L + F_EARLY, 1);
  ST(1);
  const int unpaced = __builtin_amdgcn_readfirstlane((!isA && fcg < 0) ? 1 : 0);
  v4u kr[32], vr[32];
#define LDK(i) kr[i] = __builtin_amdgcn_raw_buffer_load_b128(rk, vo, (i) * 4096, KV_AUX)
#define LDV(i) vr[i] = __builtin_amdgcn_raw_buffer_load_b128(rv, vo, (i) * 4096, KV_AUX)
#pragma unroll
  for (int i = 0; i < 32; ++i) { if (i >= WND) PACE(); LDK(i); SCHED(); }
#pragma unroll
  for (int i = 0; i < WND; ++i) { PACE(); LDV(i); SCHED(); }
  ST(2);
  lds_wait_ge(L + F_Q, 1);
  ST(3);
  const unsigned tag = lds_ld(L + F_TAG);
  const v4u q0 = *(LAS v4u*)(L + L_Q + c * 16), q1 = *(LAS v4u*)(L + L_Q + 256 + c * 16);
  float S[2][2];
#pragma unroll
  for (int j = 0; j < 2; ++j) {
    float x0[16], x1[16];
#pragma unroll
    for (int i = 0; i < 16; ++i) {
      const v4u k = kr[16 * j + i];
      float a = 0.f, b = 0.f;
#pragma unroll
      for (int e = 0; e < 4; ++e) { a = dot2(k[e], q0[e], a); b = dot2(k[e], q1[e], b); }
      x0[i] = a; x1[i] = b;
      if (i & 1) { const int n = WND + 8 * j + (i >> 1); if (n < 32) { SCHED(); PACE(); LDV(n); SCHED(); } }
    }
#define TR_STEP(HALF, CTRL, BIT) _Pragma("unroll") for (int h = 0; h < (HALF); ++h) { \
      const bool up = c & (BIT); \
      x0[h] = (up ? x0[h + (HALF)] : x0[h]) + dpp<CTRL>(up ? x0[h] : x0[h + (HALF)]); \
      x1[h] = (up ? x1[h + (HALF)] : x1[h]) + dpp<CTRL>(up ? x1[h] : x1[h + (HALF)]); }
    TR_STEP(8, ROR8, 8) TR_STEP(4, XOR7, 4) TR_STEP(2, XOR2, 2) TR_STEP(1, XOR1, 1)
#undef TR_STEP
    S[0][j] = x0[0]; S[1][j] = x1[0];
  }
  constexpr float C2 = 0.08838834764831845f * 1.4426950408889634f;
  const float m0 = xrow_max(row_max(fmaxf(S[0][0], S[0][1]))) * C2, m1 = xrow_max(row_max(fmaxf(S[1][0], S[1][1]))) * C2;
  unsigned P[2];
#pragma unroll
  for (int j = 0; j < 2; ++j) P[j] = pk2(__builtin_amdgcn_exp2f(fmaf(S[0][j], C2, -m0)), __builtin_amdgcn_exp2f(fmaf(S[1][j], C2, -m1)));
  ST(4);
  unsigned sel[4];
  {
    const unsigned hb = (c >> 3) * 2, two = hb | ((hb + 1) << 8);
    const unsigned nz = (c & 1) ? (0x0c0cu | (two << 16)) : (0x0c0c0000u | two);
#pragma unroll
    for (int d = 0; d < 4; ++d) sel[d] = ((c & 7) >> 1) == d ? nz : 0x0c0c0c0cu;
  }
  f32x4 oa = {0.f, 0.f, 0.f, 0.f}, obb = {0.f, 0.f, 0.f, 0.f};
#define PV(i, ACC) { const unsigned pb = dppu<BCAST + ((i) & 15)>(P[(i) >> 4]); \
    ACC = mfma(vr[i], v4u{__builtin_amdgcn_perm(pb, pb, sel[0]), __builtin_amdgcn_perm(pb, pb, sel[1]), __builtin_amdgcn_perm(pb, pb, sel[2]), __builtin_amdgcn_perm(pb, pb, sel[3])}, ACC); }
#define PV2(i) { constexpr int n_ = WND + 16 + (i) / 2; if constexpr (n_ < 32) { SCHED(); PACE(); LDV(n_); if constexpr (n_ == 31) { if (lane == 0) __hip_atomic_fetch_add((LAS unsigned*)(L + F_VISS), 1u, __ATOMIC_RELAXED, __HIP_MEMORY_SCOPE_WORKGROUP); } SCHED(); } PV(i, oa) PV((i) + 1, obb) }
  if constexpr (WND + 16 > 31) { if (lane == 0) __hip_atomic_fetch_add((LAS unsigned*)(L + F_VISS), 1u, __ATOMIC_RELAXED, __HIP_MEMORY_SCOPE_WORKGROUP); }
  PV2(0) PV2(2) PV2(4) PV2(6) PV2(8) PV2(10) PV2(12) PV2(14)
  const float l0 = xrow_sum(row_sum(blo(P[0]) + blo(P[1]))), l1 = xrow_sum(row_sum(bhi(P[0]) + bhi(P[1])));
  if (lane == 0) *(LAS f32x4*)(L + L_PS + w * 16) = f32x4{m0, m1, l0, l1};
  PV2(16) PV2(18) PV2(20) PV2(22) PV2(24) PV2(26) PV2(28) PV2(30)
#undef PV2
#undef PV
  ST(5);
  {
    LAS float* pw = (LAS float*)(L + L_PW + w * 1024) + (c >> 3) * 128 + r * 32 + (c & 7);
#pragma unroll
    for (int e = 0; e < 4; ++e) pw[8 * e] = oa[e] + obb[e];
  }
  asm volatile("s_waitcnt lgkmcnt(0)" ::: "memory");
  unsigned arrived = 0;
  if (lane == 0) arrived = __hip_atomic_fetch_add((LAS unsigned*)(L + F_PART), 1u, __ATOMIC_RELAXED, __HIP_MEMORY_SCOPE_WORKGROUP);
  arrived = __builtin_amdgcn_readfirstlane(arrived);
  asm volatile("" ::: "memory");
  const int hd = lane >> 5;
  if (arrived == 3) {
    float mw[4], lw[4], lam[2];
    f32x4 st[4];
#pragma unroll
    for (int k = 0; k < 4; ++k) st[k] = *(LAS f32x4*)(L + L_PS + k * 16);
#pragma unroll
    for (int k = 0; k < 4; ++k) { mw[k] = hd ? st[k][1] : st[k][0]; lw[k] = hd ? st[k][3] : st[k][2]; }
    const float M = fmaxf(fmaxf(mw[0], mw[1]), fmaxf(mw[2], mw[3]));
    float Ls = 0.f; f32x4 o = {0.f, 0.f, 0.f, 0.f};
#pragma unroll
    for (int k = 0; k < 4; ++k) { const float e = __builtin_amdgcn_exp2f(mw[k] - M); Ls = fmaf(e, lw[k], Ls); o += e * *(LAS f32x4*)(L + L_PW + k * 1024 + lane * 16); }
    const float inv = __builtin_amdgcn_rcpf(Ls), lm = M + __builtin_amdgcn_logf(Ls);
    lam[0] = __builtin_bit_cast(float, __builtin_amdgcn_readlane(__builtin_bit_cast(int, lm), 0));
    lam[1] = __builtin_bit_cast(float, __builtin_amdgcn_readlane(__builtin_bit_cast(int, lm), 32));
    const int me = kvh * 16 + gi;
    __builtin_amdgcn_raw_buffer_store_b128(v4u{pk2(o[0] * inv, o[1] * inv), tag, pk2(o[2] * inv, o[3] * inv), tag}, rws, WS_PART + me * 1024 + lane * 16, 0, AUX_SC1);
    if (lane == 0) __builtin_amdgcn_raw_buffer_store_b128(v4u{__float_as_uint(lam[0]), tag, __float_as_uint(lam[1]), tag}, rws, WS_STAT + me * 16, 0, AUX_SC1);
  }
  ST(6);
  if (!isA) { ST_FLUSH(); return; }

  {
    v4u d[4] = {}, sv = {};
    const int p0 = kvh * 16 + w * 4;
    unsigned miss = 31u;
    for (unsigned spin = 0; spin < SPIN_CAP; ++spin) {
      asm volatile("" ::: "memory");
#pragma unroll
      for (int k = 0; k < 4; ++k) if (miss & (1u << k)) d[k] = __builtin_amdgcn_raw_buffer_load_b128(rws, WS_PART + p0 * 1024 + lane * 16, k * 1024, GATHER_AUX);
      if (miss & 16u) sv = __builtin_amdgcn_raw_buffer_load_b128(rws, WS_STAT + (p0 + (lane & 3)) * 16, 0, GATHER_AUX);
#pragma unroll
      for (int k = 0; k < 4; ++k) if (__all(TAGOK(d[k]))) miss &= ~(1u << k);
      if (__all(TAGOK(sv))) miss &= ~16u;
      if (!miss) break;
      __builtin_amdgcn_s_sleep(POLL_SLEEP);
    }
    ST(7);
    const float lm = __uint_as_float(hd ? sv.z : sv.x);
    const float la[4] = {dpp<0x00>(lm), dpp<0x55>(lm), dpp<0xAA>(lm), dpp<0xFF>(lm)};
    const float M = fmaxf(fmaxf(la[0], la[1]), fmaxf(la[2], la[3]));
    float e[4], Ls = 0.f;
#pragma unroll
    for (int k = 0; k < 4; ++k) { e[k] = __builtin_amdgcn_exp2f(la[k] - M); Ls += e[k]; }
    const float inv = __builtin_amdgcn_rcpf(Ls);
    f32x4 o = {0.f, 0.f, 0.f, 0.f};
#pragma unroll
    for (int k = 0; k < 4; ++k) o += e[k] * f32x4{blo(d[k].x), bhi(d[k].x), blo(d[k].z), bhi(d[k].z)};
    *(LAS v2u*)(L + L_AO + w * AO_STR + aperm(lane * 4)) = v2u{pk2(o[0] * inv, o[1] * inv), pk2(o[2] * inv, o[3] * inv)};
  }
  asm volatile("s_waitcnt lgkmcnt(0)" ::: "memory");
  __builtin_amdgcn_s_barrier();
  asm volatile("" ::: "memory");
  {
    const int q = (lane >> 2) & 3, p = lane & 3, sw = ((2 * r + (q >> 1)) & 3) << 1;
    lds_t wt = L + L_LATE + (4 * r + q) * 128 + (((2 * w + (p >> 1)) ^ sw) << 4) + (p & 1) * 8;
    lds_t ap = L + L_AO + (lane & 3) * AO_STR + r * 16;
    v2u lo[8], hi[8]; v4u b[8];
#pragma unroll
    for (int s = 0; s < 8; ++s) { lo[s] = tr_read(wt + s * 4096); hi[s] = tr_read(wt + s * 4096 + 2048); b[s] = *(LAS v4u*)(ap + s * 64); }
    f32x4 y = {0.f, 0.f, 0.f, 0.f}, y2 = {0.f, 0.f, 0.f, 0.f};
#pragma unroll
    for (int s = 0; s < 8; s += 2) { y = mfma(v4u{lo[s].x, lo[s].y, hi[s].x, hi[s].y}, b[s], y); y2 = mfma(v4u{lo[s + 1].x, lo[s + 1].y, hi[s + 1].x, hi[s + 1].y}, b[s + 1], y2); }
    y += y2;
    const int x4 = c >> 2;
    const bool u = x4 & 1;
    const int off = x4 < 2 ? WS_SP + (gi * 16 + kvh) * 2048 + (c & 3) * 512 + (8 * w + 2 * r + x4) * 16 : WS_END;
    __builtin_amdgcn_raw_buffer_store_b128(v4u{__float_as_uint(u ? y[2] : y[0]), tag, __float_as_uint(u ? y[3] : y[1]), tag}, rws, off, 0, AUX_SC1);
  }
  ST(8);
  if (w == 0) {
    const int p = lane >> 3, lo16 = ob * 512 + (kvh & 3) * 128 + (lane & 7) * 16;
    v4u s0, s1, f0, f1;
    for (unsigned spin = 0; spin < SPIN_CAP; ++spin) {
        asm volatile("" ::: "memory");
      s0 = __builtin_amdgcn_raw_buffer_load_b128(rws, WS_SP + (gi * 16 + p) * 2048 + lo16, 0, GATHER_AUX);
      s1 = __builtin_amdgcn_raw_buffer_load_b128(rws, WS_SP + (gi * 16 + 8 + p) * 2048 + lo16, 0, GATHER_AUX);
      f0 = __builtin_amdgcn_raw_buffer_load_b128(rws, WS_FP + (gi * 9 + p) * 2048 + lo16, 0, GATHER_AUX);
      f1 = __builtin_amdgcn_raw_buffer_load_b128(rws, WS_FP + (gi * 9 + 8) * 2048 + lo16, 0, GATHER_AUX);
      const bool ok = TAGOK(s0) & TAGOK(s1) & TAGOK(f0) & TAGOK(f1);
      if (__all(ok)) break;
    }
    float sa = __uint_as_float(s0.x) + __uint_as_float(s1.x), sb = __uint_as_float(s0.z) + __uint_as_float(s1.z);
    float fa = __uint_as_float(f0.x), fb = __uint_as_float(f0.z);
    sa += dpp<ROR8>(sa); sb += dpp<ROR8>(sb); fa += dpp<ROR8>(fa); fb += dpp<ROR8>(fb);
    sa = rbf(xrow_sum(sa)); sb = rbf(xrow_sum(sb));
    fa = rbf(xrow_sum(fa) + __uint_as_float(f1.x)); fb = rbf(xrow_sum(fb) + __uint_as_float(f1.z));
    const float ga = rbf(fa * __builtin_amdgcn_rcpf(1.f + __builtin_amdgcn_exp2f(-1.4426950408889634f * fa))), gb = rbf(fb * __builtin_amdgcn_rcpf(1.f + __builtin_amdgcn_exp2f(-1.4426950408889634f * fb)));
    const unsigned res = pk2(blo(xres) + rbf(sa * ga), bhi(xres) + rbf(sb * gb));
    if (lane < 8) *(unsigned*)(out + ob * DM + ocol) = res;
    if (wg == 0 && lane < 32) __builtin_amdgcn_raw_buffer_store_b32(lds_ld(L + L_EP + lane * 4) + 1u, rws, WS_EPOCH + lane * 4, 0, AUX_SC1);
  }
  ST(9);
  ST_FLUSH();
}
}

extern "C" void kernel_launch(void* const* d_in, const int* in_sizes, int n_in, void* d_out, int out_size, void* d_ws, size_t ws_size, hipStream_t stream) {
  fused_decode<<<256, 384, 0, stream>>>((const u16*)d_in[0], (const u16*)d_in[1], (const u16*)d_in[2], (const u16*)d_in[3], (const u16*)d_in[4],
                                        (const u16*)d_in[6], (const u16*)d_in[7], (const u16*)d_in[8], (u16*)d_out, (unsigned char*)d_ws);
}
```

```cpp
#define DIAG_ROLE 0
#define DIAG_K 0
#include <hip/hip_runtime.h>
#include <hip/hip_bf16.h>
#include <stdint.h>

#define LAS __attribute__((address_space(3)))
typedef unsigned v4u __attribute__((ext_vector_type(4)));
typedef unsigned v2u __attribute__((ext_vector_type(2)));
typedef float f32x4 __attribute__((ext_vector_type(4)));
typedef short s16x4 __attribute__((ext_vector_type(4)));
typedef __bf16 bf16x2 __attribute__((ext_vector_type(2)));
typedef __bf16 bf16x8 __attribute__((ext_vector_type(8)));
typedef unsigned short u16;
typedef __amdgpu_buffer_rsrc_t rsrc_t;

namespace {
constexpr int DM = 576, SK = 8192, DH = 128, WIN = 2048, FF = 4608, QD = 4096;
constexpr int AUX_NT = 2, AUX_SC1 = 16;
#define KV_AUX 2
#define GATHER_AUX 16
#define WINDOW 8
#define Q_DELAY 420
#define H_DELAY 480
#define POLL_SLEEP 4
#define EARLY_WIN 48
constexpr int WND = WINDOW;
constexpr unsigned SPIN_CAP = 30000;

constexpr int WS_EPOCH = 0;
constexpr int WS_EARLY = 4096;
constexpr int WS_PART = WS_EARLY + 208 * 1024;
constexpr int WS_STAT = WS_PART + 256 * 1024;
constexpr int WS_FP = WS_STAT + 4096;
constexpr int WS_SP = WS_FP + 81 * 2048;
constexpr int WS_END = WS_SP + 144 * 2048;
constexpr int WS_STAMP = 8 << 20;

constexpr int L_WT = 0;
constexpr int L_LATE = 73728;
constexpr int L_AX = 106496, AX_STR = 1216;
constexpr int L_AH = L_AX + 4 * AX_STR, AH_STR = 1600;
constexpr int L_AO = L_AH + 4 * AH_STR, AO_STR = 576;
constexpr int L_Q = L_AO + 4 * AO_STR;
constexpr int L_PW = L_Q + 512;
constexpr int L_PS = L_PW + 4096;
constexpr int L_EP = L_PS + 64;
constexpr int L_FLAG = L_EP + 128;
constexpr int F_TAG = L_FLAG, F_Q = L_FLAG + 4, F_VISS = L_FLAG + 8, F_XN = L_FLAG + 12, F_PART = L_FLAG + 16, F_EARLY = L_FLAG + 20;
constexpr int L_BYTES = L_FLAG + 64;
static_assert(L_BYTES <= 160 * 1024 && L_BYTES > 80 * 1024, "one workgroup per CU");

#define ST(k) do { if ((k) == DIAG_K) { __builtin_amdgcn_sched_barrier(0); tdiag = __builtin_amdgcn_s_memrealtime(); __builtin_amdgcn_sched_barrier(0); } } while (0)
#define ST_DECL unsigned long long tdiag = 0
#define ST_FLUSH() do { if (w == DIAG_ROLE && tdiag) while (__builtin_amdgcn_s_memrealtime() - tdiag < 4000) __builtin_amdgcn_s_sleep(1); } while (0)

__device__ __forceinline__ rsrc_t mk_rsrc(const void* p, int bytes) { return __builtin_amdgcn_make_buffer_rsrc(const_cast<void*>(p), 0, bytes, 0x00020000); }
__device__ __forceinline__ float blo(unsigned u) { return __uint_as_float(u << 16); }
__device__ __forceinline__ float bhi(unsigned u) { return __uint_as_float(u & 0xffff0000u); }
__device__ __forceinline__ unsigned pk2(float lo, float hi) {
  bf16x2 r; r[0] = (__bf16)lo; r[1] = (__bf16)hi; return __builtin_bit_cast(unsigned, r);
}
__device__ __forceinline__ float rbf(float f) { return (float)(__bf16)f; }
__device__ __forceinline__ float dot2(unsigned k, unsigned q, float acc) {
  return __builtin_amdgcn_fdot2_f32_bf16(__builtin_bit_cast(bf16x2, k), __builtin_bit_cast(bf16x2, q), acc, false);
}
template <int CTRL> __device__ __forceinline__ float dpp(float x) {
  return __builtin_bit_cast(float, __builtin_amdgcn_mov_dpp(__builtin_bit_cast(int, x), CTRL, 0xf, 0xf, true));
}
template <int CTRL> __device__ __forceinline__ unsigned dppu(unsigned x) { return (unsigned)__builtin_amdgcn_mov_dpp((int)x, CTRL, 0xf, 0xf, true); }
constexpr int XOR1 = 0xB1, XOR2 = 0x4E, XOR7 = 0x141, ROR8 = 0x128, ROR4 = 0x124, ROR2 = 0x122, ROR1 = 0x121, BCAST = 0x150;
__device__ __forceinline__ float xrow_max(float x) {
  auto s = __builtin_amdgcn_permlane16_swap(__float_as_uint(x), __float_as_uint(x), false, false);
  x = fmaxf(__uint_as_float(s[0]), __uint_as_float(s[1]));
  auto t = __builtin_amdgcn_permlane32_swap(__float_as_uint(x), __float_as_uint(x), false, false);
  return fmaxf(__uint_as_float(t[0]), __uint_as_float(t[1]));
}
__device__ __forceinline__ float xrow_sum(float x) {
  auto s = __builtin_amdgcn_permlane16_swap(__float_as_uint(x), __float_as_uint(x), false, false);
  x = __uint_as_float(s[0]) + __uint_as_float(s[1]);
  auto t = __builtin_amdgcn_permlane32_swap(__float_as_uint(x), __float_as_uint(x), false, false);
  return __uint_as_float(t[0]) + __uint_as_float(t[1]);
}
__device__ __forceinline__ float row_sum(float x) { x += dpp<ROR8>(x); x += dpp<ROR4>(x); x += dpp<ROR2>(x); x += dpp<ROR1>(x); return x; }
__device__ __forceinline__ float row_max(float x) { x = fmaxf(x, dpp<ROR8>(x)); x = fmaxf(x, dpp<ROR4>(x)); x = fmaxf(x, dpp<ROR2>(x)); x = fmaxf(x, dpp<ROR1>(x)); return x; }
__device__ __forceinline__ f32x4 mfma(v4u a, v4u b, f32x4 c) {
  return __builtin_amdgcn_mfma_f32_16x16x32_bf16(__builtin_bit_cast(bf16x8, a), __builtin_bit_cast(bf16x8, b), c, 0, 0, 0);
}
#define TAGOK(v) ((((v).y ^ tag) | ((v).w ^ tag)) == 0u)
#define TOUCH(x) asm volatile("" ::"v"(x))
#define PACE() asm volatile("s_cmp_lg_u32 %0, 0\n\ts_cbranch_scc1 .Lpace%=\n\ts_waitcnt vmcnt(%1)\n.Lpace%=:" ::"s"(unpaced), "i"(WND - 1) : "scc")
#define SCHED() __builtin_amdgcn_sched_barrier(0)

typedef LAS unsigned char* lds_t;
__device__ __forceinline__ v2u tr_read(lds_t p) { return __builtin_bit_cast(v2u, __builtin_amdgcn_ds_read_tr16_b64_v4i16((LAS s16x4*)p)); }
__device__ __forceinline__ unsigned lds_ld(lds_t p) { return __hip_atomic_load((LAS unsigned*)p, __ATOMIC_RELAXED, __HIP_MEMORY_SCOPE_WORKGROUP); }
__device__ __forceinline__ void lds_st(lds_t p, unsigned v) { __hip_atomic_store((LAS unsigned*)p, v, __ATOMIC_RELAXED, __HIP_MEMORY_SCOPE_WORKGROUP); }
__device__ __forceinline__ void lds_wait_ge(lds_t p, unsigned want) {
  while ((unsigned)__builtin_amdgcn_readfirstlane(lds_ld(p)) < want) __builtin_amdgcn_s_sleep(1);
  asm volatile("" ::: "memory");
}
__device__ __forceinline__ void dma_8pieces(rsrc_t r, int voff, int& soff, int step, unsigned ldsoff) {
#define DMA_NEXT "s_add_u32 m0, m0, 0x400\n\ts_add_u32 %0, %0, %4\n\tbuffer_load_dwordx4 %2, %3, %0 offen nt lds\n\t"
  asm volatile("s_mov_b32 m0, %1\n\ts_nop 0\n\tbuffer_load_dwordx4 %2, %3, %0 offen nt lds\n\t" DMA_NEXT DMA_NEXT DMA_NEXT DMA_NEXT DMA_NEXT DMA_NEXT DMA_NEXT
               "s_add_u32 %0, %0, %4"
               : "+s"(soff) : "s"(ldsoff), "v"(voff), "s"(r), "s"(step) : "memory", "scc");
#undef DMA_NEXT
}
__device__ __forceinline__ int aperm(int k) { const int g = (k >> 2) & 7; return (k >> 5) * 64 + (g & 3) * 16 + (g >> 2) * 8; }

__device__ __forceinline__ void gemv_2steps(lds_t wt, const int (&tro)[4], lds_t ap, f32x4 (&acc)[4]) {
  v2u lo[2][4], hi[2][4]; v4u b[2];
#pragma unroll
  for (int s = 0; s < 2; ++s) {
    b[s] = *(LAS v4u*)(ap + s * 64);
#pragma unroll
    for (int t = 0; t < 4; ++t) { lo[s][t] = tr_read(wt + tro[t] + s * 4096); hi[s][t] = tr_read(wt + tro[t] + s * 4096 + 2048); }
  }
#pragma unroll
  for (int s = 0; s < 2; ++s)
#pragma unroll
    for (int t = 0; t < 4; ++t) acc[t] = mfma(v4u{lo[s][t].x, lo[s][t].y, hi[s][t].x, hi[s][t].y}, b[s], acc[t]);
}
__device__ __forceinline__ f32x4 sel2(bool c, f32x4 a, f32x4 b) { return f32x4{c ? b[0] : a[0], c ? b[1] : a[1], c ? b[2] : a[2], c ? b[3] : a[3]}; }

template <int N> __device__ __forceinline__ void wait_vm() { asm volatile("s_waitcnt vmcnt(%0)" ::"i"(N) : "memory"); }

__global__ void __launch_bounds__(384, 2)
fused_decode(const u16* __restrict__ x, const u16* __restrict__ Kc, const u16* __restrict__ Vc, const u16* __restrict__ wq, const u16* __restrict__ wo,
             const u16* __restrict__ wgate, const u16* __restrict__ wup, const u16* __restrict__ wdn, u16* __restrict__ out, unsigned char* __restrict__ ws) {
  ST_DECL;
  ST(0);
  __shared__ __attribute__((aligned(16))) unsigned char smem[L_BYTES];
  lds_t L = (lds_t)smem;
  const int lane = threadIdx.x & 63, w = __builtin_amdgcn_readfirstlane(threadIdx.x >> 6);
  const int wg = blockIdx.x, xcd = wg & 7, idx = wg >> 3;
  const int kvh = xcd * 2 + (idx >> 4), gi = idx & 15, bat = gi >> 2, quarter = gi & 3;
  const bool isA = gi < 9;
  int eu = -1, fcg = -1, ftile = 0;
  bool kfirst = false;
  {
    int n = -1;
    if (isA) n = kvh * 9 + gi;
    else {
      const int o = (idx >> 4) * 7 + (gi - 9);
      int hx = -1;
      if (xcd & 1) {
        if (o < 7) hx = 53 + 7 * (xcd >> 1) + o;
        else { const int j = 7 * (xcd >> 1) + (o - 7); if (j < 26) eu = 8 * j + 3; else n = 154 + j; }
      } else if (o < 13 || xcd == 6) hx = 13 * (xcd >> 1) + o;
      else kfirst = true;
      if (hx >= 45) { const int j = hx - 45; n = 144 + j; fcg = j >> 2; ftile = 5 + (j & 3); }
      else if (hx >= 0) { fcg = hx / 5; ftile = hx - 5 * fcg; }
    }
    if (n >= 0) { const int q7 = n / 7, r7 = n - 7 * q7; eu = 8 * q7 + r7 + (r7 >= 3); }
  }
  const bool isB = fcg >= 0 && ftile < 5, isC = fcg >= 0 && ftile >= 5;
  const int frow0 = ftile < 5 ? ftile * 768 : 3840 + (ftile - 5) * 192;
  const rsrc_t rws = mk_rsrc(ws, WS_END);

  if (w == 4) {
    __builtin_amdgcn_s_setprio(3);
    const unsigned long long t0 = __builtin_amdgcn_s_memrealtime();
    const int prow = lane >> 3, pch = (lane & 7) ^ (((prow >> 1) & 3) << 1);
    {
      const u16* base; int stride, n;
      if (eu >= 136) { base = wup + (eu - 136) * 64; stride = FF * 2; n = 72; }
      else if (eu >= 64) { base = wgate + (eu - 64) * 64; stride = FF * 2; n = 72; }
      else if (eu >= 0) { base = wq + eu * 64; stride = QD * 2; n = 72; }
      else { base = wdn + (size_t)frow0 * DM + (fcg < 0 ? 0 : fcg) * 64; stride = DM * 2; n = isB ? 64 : 0; }
      const rsrc_t r = mk_rsrc(base, n > 0 ? (n * 8 - 1) * stride + 128 : 0);
      const int voff = prow * stride + pch * 16;
      int soff = 0;
      if (n > 0) dma_8pieces(r, voff, soff, 8 * stride, (unsigned)(size_t)(L + L_WT));
      __builtin_amdgcn_s_barrier();
#pragma unroll 1
      for (int d = 8; d < n; d += 8) {
        wait_vm<EARLY_WIN>();
        dma_8pieces(r, voff, soff, 8 * stride, (unsigned)(size_t)(L + L_WT) + d * 1024);
      }
    }
    if (lane == 0) lds_st(L + F_EARLY, 1u);
    ST(1);
    lds_wait_ge(L + F_XN, 1);
    const unsigned tag = lds_ld(L + F_TAG);
    int tro[4];
    {
      const int g = lane >> 4, q = (lane >> 2) & 3, p = lane & 3, sw = ((2 * g + (q >> 1)) & 3) << 1;
#pragma unroll
      for (int t = 0; t < 4; ++t) tro[t] = (4 * g + q) * 128 + (((2 * t + (p >> 1)) ^ sw) << 4) + (p & 1) * 8;
    }
    const int g4 = lane >> 4, c = lane & 15;
    ST(2);
    if (eu >= 0) {
      f32x4 acc[4] = {};
      lds_t ap = L + L_AX + (lane & 3) * AX_STR + g4 * 16;
#define EARLY_STEP(j) wait_vm<(64 - 8 * (j)) < 56 ? (64 - 8 * (j)) : 56>(); gemv_2steps(L + L_WT + (j) * 8192, tro, ap + (j) * 128, acc);
      EARLY_STEP(0) EARLY_STEP(1) EARLY_STEP(2) EARLY_STEP(3) EARLY_STEP(4) EARLY_STEP(5) EARLY_STEP(6) EARLY_STEP(7) EARLY_STEP(8)
#undef EARLY_STEP
      const f32x4 y = sel2(c & 8, sel2(c & 4, acc[0], acc[1]), sel2(c & 4, acc[2], acc[3]));
      __builtin_amdgcn_raw_buffer_store_b128(v4u{pk2(y[0], y[1]), tag, pk2(y[2], y[3]), tag}, rws, WS_EARLY + eu * 1024 + (c & 3) * 256 + ((c >> 2) * 4 + g4) * 16, 0, AUX_SC1);
    }
    ST(3);
    f32x4 facc[4] = {};
    lds_t hp = L + L_AH + (lane & 3) * AH_STR + g4 * 16;
    if (fcg >= 0) {
      while (__builtin_amdgcn_s_memrealtime() - t0 < H_DELAY) __builtin_amdgcn_s_sleep(2);
      const int ncg = isB ? 12 : 3;
      const int off = WS_EARLY + (64 + (frow0 >> 6)) * 1024 + lane * 16;
      v4u g[12], u[12];
      unsigned miss = (1u << ncg) - 1u;
      for (unsigned spin = 0; spin < SPIN_CAP; ++spin) {
        asm volatile("" ::: "memory");
#pragma unroll
        for (int k = 0; k < 12; k += 3)
          if (miss & (7u << k)) {
#pragma unroll
            for (int e = k; e < k + 3; ++e) { g[e] = __builtin_amdgcn_raw_buffer_load_b128(rws, off, e * 1024, GATHER_AUX); u[e] = __builtin_amdgcn_raw_buffer_load_b128(rws, off, (72 + e) * 1024, GATHER_AUX); }
          }
#pragma unroll
        for (int k = 0; k < 12; k += 3)
          if (miss & (7u << k)) {
            const bool ok = TAGOK(g[k]) & TAGOK(u[k]) & TAGOK(g[k + 1]) & TAGOK(u[k + 1]) & TAGOK(g[k + 2]) & TAGOK(u[k + 2]);
            if (__all(ok)) {
              miss &= ~(7u << k);
#pragma unroll
              for (int e = k; e < k + 3; ++e) {
                const float gv[4] = {blo(g[e].x), bhi(g[e].x), blo(g[e].z), bhi(g[e].z)}, uv[4] = {blo(u[e].x), bhi(u[e].x), blo(u[e].z), bhi(u[e].z)};
                float hv[4];
#pragma unroll
                for (int i = 0; i < 4; ++i) {
                  const float a = gv[i], inner = 0.7978845608028654f * fmaf(0.044715f * a * a, a, a);
                  hv[i] = rbf(a * __builtin_amdgcn_rcpf(1.f + __builtin_amdgcn_exp2f(-2.8853900817779268f * inner))) * uv[i];
                }
                *(LAS v2u*)(L + L_AH + g4 * AH_STR + aperm(e * 64 + c * 4)) = v2u{pk2(hv[0], hv[1]), pk2(hv[2], hv[3])};
              }
            }
          }
        if (!miss) break;
      }
      ST(8);
      if (isB) {
        wait_vm<0>();
#pragma unroll 1
        for (int j = 0; j < 8; ++j) gemv_2steps(L + L_WT + j * 8192, tro, hp + j * 128, facc);
        hp += 1024;
      }
      ST(9);
    }
    if (isA || fcg >= 0) {
      lds_wait_ge(L + F_VISS, 4);
      ST(4);
      const u16* base = isA ? wo + (size_t)kvh * 256 * DM + gi * 64 : wdn + (size_t)(frow0 + (isB ? 512 : 0)) * DM + fcg * 64;
      const int n = isC ? 24 : 32;
      const rsrc_t r = mk_rsrc(base, (n * 8 - 1) * (DM * 2) + 128);
      const int voff = prow * (DM * 2) + pch * 16;
      int soff = 0;
#pragma unroll 1
      for (int d = 0; d < n; d += 8) dma_8pieces(r, voff, soff, 8 * (DM * 2), (unsigned)(size_t)(L + L_LATE) + d * 1024);
      ST(5);
      wait_vm<0>();
      ST(6);
      if (isA) {
        __builtin_amdgcn_s_barrier();
      } else {
#pragma unroll 1
        for (int j = 0; j < n / 8; ++j) gemv_2steps(L + L_LATE + j * 8192, tro, hp + j * 128, facc);
        const int x4 = c >> 2;
        const f32x4 y0 = sel2(x4 & 2, facc[0], facc[1]), y1 = sel2(x4 & 2, facc[2], facc[3]);
        const bool u = x4 & 1;
        const int off = WS_FP + (fcg * 9 + ftile) * 2048 + (c & 3) * 512 + ((x4 >> 1) * 8 + 2 * g4 + (x4 & 1)) * 16;
        __builtin_amdgcn_raw_buffer_store_b128(v4u{__float_as_uint(u ? y0[2] : y0[0]), tag, __float_as_uint(u ? y0[3] : y0[1]), tag}, rws, off, 0, AUX_SC1);
        __builtin_amdgcn_raw_buffer_store_b128(v4u{__float_as_uint(u ? y1[2] : y1[0]), tag, __float_as_uint(u ? y1[3] : y1[1]), tag}, rws, off + 256, 0, AUX_SC1);
      }
    }
    ST(7);
    ST_FLUSH();
    return;
  }

  if (w == 5) {
    __builtin_amdgcn_s_setprio(3);
    const unsigned long long t0 = __builtin_amdgcn_s_memrealtime();
    const rsrc_t rx = mk_rsrc(x, 4 * DM * 2);
    const int r = lane >> 4, c = lane & 15;
    v4u xv[5];
#pragma unroll
    for (int i = 0; i < 5; ++i) xv[i] = __builtin_amdgcn_raw_buffer_load_b128(rx, (i < 4 || c < 8) ? r * (DM * 2) + (c + 16 * i) * 16 : 0x10000, 0, 0);
    const unsigned ep = __builtin_amdgcn_raw_buffer_load_b32(rws, WS_EPOCH + (lane & 31) * 4, 0, AUX_SC1);
    __builtin_amdgcn_s_barrier();
    {
      float s = 0.f;
#pragma unroll
      for (int i = 0; i < 5; ++i)
#pragma unroll
        for (int k = 0; k < 4; ++k) s += blo(xv[i][k]) + bhi(xv[i][k]);
      const float mean = row_sum(s) * (1.f / DM);
      float q = 0.f;
#pragma unroll
      for (int i = 0; i < 5; ++i)
#pragma unroll
        for (int k = 0; k < 4; ++k) {
          const float a = blo(xv[i][k]) - mean, b = bhi(xv[i][k]) - mean;
          const float d = fmaf(a, a, b * b);
          q += (i < 4 || c < 8) ? d : 0.f;
        }
      const float var = rbf(row_sum(q) * (1.f / DM)), mb = rbf(mean);
      const float den = rbf(sqrtf(rbf(var + 1e-5f)));
      const float inv = __builtin_amdgcn_rcpf(den);
#pragma unroll
      for (int i = 0; i < 5; ++i) {
        unsigned o[4];
#pragma unroll
        for (int k = 0; k < 4; ++k) {
          const unsigned d = pk2(blo(xv[i][k]) - mb, bhi(xv[i][k]) - mb);
          o[k] = pk2(blo(d) * inv, bhi(d) * inv);
        }
        if (i < 4 || c < 8) {
          const int k0 = (c + 16 * i) * 8;
          *(LAS v2u*)(L + L_AX + r * AX_STR + aperm(k0)) = v2u{o[0], o[1]};
          *(LAS v2u*)(L + L_AX + r * AX_STR + aperm(k0 + 4)) = v2u{o[2], o[3]};
        }
      }
    }
    unsigned tag = ep;
    tag += dppu<ROR8>(tag); tag += dppu<ROR4>(tag); tag += dppu<ROR2>(tag); tag += dppu<ROR1>(tag);
    tag = (unsigned)__builtin_amdgcn_readlane(tag, 0) + (unsigned)__builtin_amdgcn_readlane(tag, 16) + 0x9E3779B9u;
    if (lane < 32) lds_st(L + L_EP + lane * 4, ep);
    if (lane == 0) lds_st(L + F_TAG, tag);
    asm volatile("s_waitcnt lgkmcnt(0)" ::: "memory");
    if (lane == 0) lds_st(L + F_XN, 1u);
    ST(1);
    while (__builtin_amdgcn_s_memrealtime() - t0 < Q_DELAY) __builtin_amdgcn_s_sleep(2);
    ST(2);
    {
      const int off = WS_EARLY + (kvh * 4 + r) * 1024 + bat * 256 + c * 16;
      v4u g;
      for (unsigned spin = 0; spin < SPIN_CAP; ++spin) {
        asm volatile("" ::: "memory");
        g = __builtin_amdgcn_raw_buffer_load_b128(rws, off, 0, GATHER_AUX);
        if (__all(TAGOK(g))) break;
      }
      *(LAS v2u*)(L + L_Q + lane * 8) = v2u{g.x, g.z};
      asm volatile("s_waitcnt lgkmcnt(0)" ::: "memory");
      if (lane == 0) lds_st(L + F_Q, 1u);
    }
    ST(3);
    ST(4);
    ST_FLUSH();
    return;
  }

  const int r = lane >> 4, c = lane & 15;
  const size_t kvbase = ((size_t)(bat * 16 + kvh) * SK + (SK - WIN) + quarter * 512) * DH;
  const rsrc_t rk = mk_rsrc(Kc + kvbase, 131072), rv = mk_rsrc(Vc + kvbase, 131072);
  const int vo = lane * 16 + w * 1024;
  unsigned xres = 0;
  const int ob = kvh >> 2, ocol = gi * 64 + (kvh & 3) * 16 + (lane & 7) * 2;
  if (w == 0) {
    if (lane < 8) lds_st(L + L_FLAG + lane * 4, 0u);
    asm volatile("s_waitcnt lgkmcnt(0)" ::: "memory");
    if (isA) xres = *(const unsigned*)(x + ob * DM + ocol);
  }
  __builtin_amdgcn_s_barrier();
  asm volatile("" ::: "memory");
  if (!kfirst) lds_wait_ge(L + F_EARLY, 1);
  ST(1);
  const int unpaced = __builtin_amdgcn_readfirstlane((!isA && fcg < 0) ? 1 : 0);
  v4u kr[32], vr[32];
#define LDK(i) kr[i] = __builtin_amdgcn_raw_buffer_load_b128(rk, vo, (i) * 4096, KV_AUX)
#define LDV(i) vr[i] = __builtin_amdgcn_raw_buffer_load_b128(rv, vo, (i) * 4096, KV_AUX)
#pragma unroll
  for (int i = 0; i < 32; ++i) { if (i >= WND) PACE(); LDK(i); SCHED(); }
#pragma unroll
  for (int i = 0; i < WND; ++i) { PACE(); LDV(i); SCHED(); }
  ST(2);
  lds_wait_ge(L + F_Q, 1);
  ST(3);
  const unsigned tag = lds_ld(L + F_TAG);
  const v4u q0 = *(LAS v4u*)(L + L_Q + c * 16), q1 = *(LAS v4u*)(L + L_Q + 256 + c * 16);
  float S[2][2];
#pragma unroll
  for (int j = 0; j < 2; ++j) {
    float x0[16], x1[16];
#pragma unroll
    for (int i = 0; i < 16; ++i) {
      const v4u k = kr[16 * j + i];
      float a = 0.f, b = 0.f;
#pragma unroll
      for (int e = 0; e < 4; ++e) { a = dot2(k[e], q0[e], a); b = dot2(k[e], q1[e], b); }
      x0[i] = a; x1[i] = b;
      if (i & 1) { const int n = WND + 8 * j + (i >> 1); if (n < 32) { SCHED(); PACE(); LDV(n); SCHED(); } }
    }
#define TR_STEP(HALF, CTRL, BIT) _Pragma("unroll") for (int h = 0; h < (HALF); ++h) { \
      const bool up = c & (BIT); \
      x0[h] = (up ? x0[h + (HALF)] : x0[h]) + dpp<CTRL>(up ? x0[h] : x0[h + (HALF)]); \
      x1[h] = (up ? x1[h + (HALF)] : x1[h]) + dpp<CTRL>(up ? x1[h] : x1[h + (HALF)]); }
    TR_STEP(8, ROR8, 8) TR_STEP(4, XOR7, 4) TR_STEP(2, XOR2, 2) TR_STEP(1, XOR1, 1)
#undef TR_STEP
    S[0][j] = x0[0]; S[1][j] = x1[0];
  }
  constexpr float C2 = 0.08838834764831845f * 1.4426950408889634f;
  const float m0 = xrow_max(row_max(fmaxf(S[0][0], S[0][1]))) * C2, m1 = xrow_max(row_max(fmaxf(S[1][0], S[1][1]))) * C2;
  unsigned P[2];
#pragma unroll
  for (int j = 0; j < 2; ++j) P[j] = pk2(__builtin_amdgcn_exp2f(fmaf(S[0][j], C2, -m0)), __builtin_amdgcn_exp2f(fmaf(S[1][j], C2, -m1)));
  ST(4);
  unsigned sel[4];
  {
    const unsigned hb = (c >> 3) * 2, two = hb | ((hb + 1) << 8);
    const unsigned nz = (c & 1) ? (0x0c0cu | (two << 16)) : (0x0c0c0000u | two);
#pragma unroll
    for (int d = 0; d < 4; ++d) sel[d] = ((c & 7) >> 1) == d ? nz : 0x0c0c0c0cu;
  }
  f32x4 oa = {0.f, 0.f, 0.f, 0.f}, obb = {0.f, 0.f, 0.f, 0.f};
#define PV(i, ACC) { const unsigned pb = dppu<BCAST + ((i) & 15)>(P[(i) >> 4]); \
    ACC = mfma(vr[i], v4u{__builtin_amdgcn_perm(pb, pb, sel[0]), __builtin_amdgcn_perm(pb, pb, sel[1]), __builtin_amdgcn_perm(pb, pb, sel[2]), __builtin_amdgcn_perm(pb, pb, sel[3])}, ACC); }
#define PV2(i) { constexpr int n_ = WND + 16 + (i) / 2; if constexpr (n_ < 32) { SCHED(); PACE(); LDV(n_); if constexpr (n_ == 31) { if (lane == 0) __hip_atomic_fetch_add((LAS unsigned*)(L + F_VISS), 1u, __ATOMIC_RELAXED, __HIP_MEMORY_SCOPE_WORKGROUP); } SCHED(); } PV(i, oa) PV((i) + 1, obb) }
  if constexpr (WND + 16 > 31) { if (lane == 0) __hip_atomic_fetch_add((LAS unsigned*)(L + F_VISS), 1u, __ATOMIC_RELAXED, __HIP_MEMORY_SCOPE_WORKGROUP); }
  PV2(0) PV2(2) PV2(4) PV2(6) PV2(8) PV2(10) PV2(12) PV2(14)
  const float l0 = xrow_sum(row_sum(blo(P[0]) + blo(P[1]))), l1 = xrow_sum(row_sum(bhi(P[0]) + bhi(P[1])));
  if (lane == 0) *(LAS f32x4*)(L + L_PS + w * 16) = f32x4{m0, m1, l0, l1};
  PV2(16) PV2(18) PV2(20) PV2(22) PV2(24) PV2(26) PV2(28) PV2(30)
#undef PV2
#undef PV
  TOUCH(oa); TOUCH(obb);
  ST(5);
  {
    LAS float* pw = (LAS float*)(L + L_PW + w * 1024) + (c >> 3) * 128 + r * 32 + (c & 7);
#pragma unroll
    for (int e = 0; e < 4; ++e) pw[8 * e] = oa[e] + obb[e];
  }
  asm volatile("s_waitcnt lgkmcnt(0)" ::: "memory");
  unsigned arrived = 0;
  if (lane == 0) arrived = __hip_atomic_fetch_add((LAS unsigned*)(L + F_PART), 1u, __ATOMIC_RELAXED, __HIP_MEMORY_SCOPE_WORKGROUP);
  arrived = __builtin_amdgcn_readfirstlane(arrived);
  asm volatile("" ::: "memory");
  const int hd = lane >> 5;
  if (arrived == 3) {
    float mw[4], lw[4], lam[2];
    f32x4 st[4];
#pragma unroll
    for (int k = 0; k < 4; ++k) st[k] = *(LAS f32x4*)(L + L_PS + k * 16);
#pragma unroll
    for (int k = 0; k < 4; ++k) { mw[k] = hd ? st[k][1] : st[k][0]; lw[k] = hd ? st[k][3] : st[k][2]; }
    const float M = fmaxf(fmaxf(mw[0], mw[1]), fmaxf(mw[2], mw[3]));
    float Ls = 0.f; f32x4 o = {0.f, 0.f, 0.f, 0.f};
#pragma unroll
    for (int k = 0; k < 4; ++k) { const float e = __builtin_amdgcn_exp2f(mw[k] - M); Ls = fmaf(e, lw[k], Ls); o += e * *(LAS f32x4*)(L + L_PW + k * 1024 + lane * 16); }
    const float inv = __builtin_amdgcn_rcpf(Ls), lm = M + __builtin_amdgcn_logf(Ls);
    lam[0] = __builtin_bit_cast(float, __builtin_amdgcn_readlane(__builtin_bit_cast(int, lm), 0));
    lam[1] = __builtin_bit_cast(float, __builtin_amdgcn_readlane(__builtin_bit_cast(int, lm), 32));
    const int me = kvh * 16 + gi;
    __builtin_amdgcn_raw_buffer_store_b128(v4u{pk2(o[0] * inv, o[1] * inv), tag, pk2(o[2] * inv, o[3] * inv), tag}, rws, WS_PART + me * 1024 + lane * 16, 0, AUX_SC1);
    if (lane == 0) __builtin_amdgcn_raw_buffer_store_b128(v4u{__float_as_uint(lam[0]), tag, __float_as_uint(lam[1]), tag}, rws, WS_STAT + me * 16, 0, AUX_SC1);
  }
  ST(6);
  if (!isA) { ST_FLUSH(); return; }

  {
    v4u d[4] = {}, sv = {};
    const int p0 = kvh * 16 + w * 4;
    unsigned miss = 31u;
    for (unsigned spin = 0; spin < SPIN_CAP; ++spin) {
      asm volatile("" ::: "memory");
#pragma unroll
      for (int k = 0; k < 4; ++k) if (miss & (1u << k)) d[k] = __builtin_amdgcn_raw_buffer_load_b128(rws, WS_PART + p0 * 1024 + lane * 16, k * 1024, GATHER_AUX);
      if (miss & 16u) sv = __builtin_amdgcn_raw_buffer_load_b128(rws, WS_STAT + (p0 + (lane & 3)) * 16, 0, GATHER_AUX);
#pragma unroll
      for (int k = 0; k < 4; ++k) if (__all(TAGOK(d[k]))) miss &= ~(1u << k);
      if (__all(TAGOK(sv))) miss &= ~16u;
      if (!miss) break;
      __builtin_amdgcn_s_sleep(POLL_SLEEP);
    }
    ST(7);
    const float lm = __uint_as_float(hd ? sv.z : sv.x);
    const float la[4] = {dpp<0x00>(lm), dpp<0x55>(lm), dpp<0xAA>(lm), dpp<0xFF>(lm)};
    const float M = fmaxf(fmaxf(la[0], la[1]), fmaxf(la[2], la[3]));
    float e[4], Ls = 0.f;
#pragma unroll
    for (int k = 0; k < 4; ++k) { e[k] = __builtin_amdgcn_exp2f(la[k] - M); Ls += e[k]; }
    const float inv = __builtin_amdgcn_rcpf(Ls);
    f32x4 o = {0.f, 0.f, 0.f, 0.f};
#pragma unroll
    for (int k = 0; k < 4; ++k) o += e[k] * f32x4{blo(d[k].x), bhi(d[k].x), blo(d[k].z), bhi(d[k].z)};
    *(LAS v2u*)(L + L_AO + w * AO_STR + aperm(lane * 4)) = v2u{pk2(o[0] * inv, o[1] * inv), pk2(o[2] * inv, o[3] * inv)};
  }
  asm volatile("s_waitcnt lgkmcnt(0)" ::: "memory");
  __builtin_amdgcn_s_barrier();
  asm volatile("" ::: "memory");
  {
    const int q = (lane >> 2) & 3, p = lane & 3, sw = ((2 * r + (q >> 1)) & 3) << 1;
    lds_t wt = L + L_LATE + (4 * r + q) * 128 + (((2 * w + (p >> 1)) ^ sw) << 4) + (p & 1) * 8;
    lds_t ap = L + L_AO + (lane & 3) * AO_STR + r * 16;
    v2u lo[8], hi[8]; v4u b[8];
#pragma unroll
    for (int s = 0; s < 8; ++s) { lo[s] = tr_read(wt + s * 4096); hi[s] = tr_read(wt + s * 4096 + 2048); b[s] = *(LAS v4u*)(ap + s * 64); }
    f32x4 y = {0.f, 0.f, 0.f, 0.f}, y2 = {0.f, 0.f, 0.f, 0.f};
#pragma unroll
    for (int s = 0; s < 8; s += 2) { y = mfma(v4u{lo[s].x, lo[s].y, hi[s].x, hi[s].y}, b[s], y); y2 = mfma(v4u{lo[s + 1].x, lo[s + 1].y, hi[s + 1].x, hi[s + 1].y}, b[s + 1], y2); }
    y += y2;
    const int x4 = c >> 2;
    const bool u = x4 & 1;
    const int off = x4 < 2 ? WS_SP + (gi * 16 + kvh) * 2048 + (c & 3) * 512 + (8 * w + 2 * r + x4) * 16 : WS_END;
    __builtin_amdgcn_raw_buffer_store_b128(v4u{__float_as_uint(u ? y[2] : y[0]), tag, __float_as_uint(u ? y[3] : y[1]), tag}, rws, off, 0, AUX_SC1);
  }
  ST(8);
  if (w == 0) {
    const int p = lane >> 3, lo16 = ob * 512 + (kvh & 3) * 128 + (lane & 7) * 16;
    v4u s0, s1, f0, f1;
    for (unsigned spin = 0; spin < SPIN_CAP; ++spin) {
        asm volatile("" ::: "memory");
      s0 = __builtin_amdgcn_raw_buffer_load_b128(rws, WS_SP + (gi * 16 + p) * 2048 + lo16, 0, GATHER_AUX);
      s1 = __builtin_amdgcn_raw_buffer_load_b128(rws, WS_SP + (gi * 16 + 8 + p) * 2048 + lo16, 0, GATHER_AUX);
      f0 = __builtin_amdgcn_raw_buffer_load_b128(rws, WS_FP + (gi * 9 + p) * 2048 + lo16, 0, GATHER_AUX);
      f1 = __builtin_amdgcn_raw_buffer_load_b128(rws, WS_FP + (gi * 9 + 8) * 2048 + lo16, 0, GATHER_AUX);
      const bool ok = TAGOK(s0) & TAGOK(s1) & TAGOK(f0) & TAGOK(f1);
      if (__all(ok)) break;
    }
    float sa = __uint_as_float(s0.x) + __uint_as_float(s1.x), sb = __uint_as_float(s0.z) + __uint_as_float(s1.z);
    float fa = __uint_as_float(f0.x), fb = __uint_as_float(f0.z);
    sa += dpp<ROR8>(sa); sb += dpp<ROR8>(sb); fa += dpp<ROR8>(fa); fb += dpp<ROR8>(fb);
    sa = rbf(xrow_sum(sa)); sb = rbf(xrow_sum(sb));
    fa = rbf(xrow_sum(fa) + __uint_as_float(f1.x)); fb = rbf(xrow_sum(fb) + __uint_as_float(f1.z));
    const float ga = rbf(fa * __builtin_amdgcn_rcpf(1.f + __builtin_amdgcn_exp2f(-1.4426950408889634f * fa))), gb = rbf(fb * __builtin_amdgcn_rcpf(1.f + __builtin_amdgcn_exp2f(-1.4426950408889634f * fb)));
    const unsigned res = pk2(blo(xres) + rbf(sa * ga), bhi(xres) + rbf(sb * gb));
    if (lane < 8) *(unsigned*)(out + ob * DM + ocol) = res;
    if (wg == 0 && lane < 32) __builtin_amdgcn_raw_buffer_store_b32(lds_ld(L + L_EP + lane * 4) + 1u, rws, WS_EPOCH + lane * 4, 0, AUX_SC1);
  }
  ST(9);
  ST_FLUSH();
}
}

extern "C" void kernel_launch(void* const* d_in, const int* in_sizes, int n_in, void* d_out, int out_size, void* d_ws, size_t ws_size, hipStream_t stream) {
  fused_decode<<<256, 384, 0, stream>>>((const u16*)d_in[0], (const u16*)d_in[1], (const u16*)d_in[2], (const u16*)d_in[3], (const u16*)d_in[4],
                                        (const u16*)d_in[6], (const u16*)d_in[7], (const u16*)d_in[8], (u16*)d_out, (unsigned char*)d_ws);
}
```

```cpp
#include <hip/hip_runtime.h>
#include <hip/hip_bf16.h>
#include <stdint.h>

#define LAS __attribute__((address_space(3)))
typedef unsigned v4u __attribute__((ext_vector_type(4)));
typedef unsigned v2u __attribute__((ext_vector_type(2)));
typedef float f32x4 __attribute__((ext_vector_type(4)));
typedef short s16x4 __attribute__((ext_vector_type(4)));
typedef __bf16 bf16x2 __attribute__((ext_vector_type(2)));
typedef __bf16 bf16x8 __attribute__((ext_vector_type(8)));
typedef unsigned short u16;
typedef __amdgpu_buffer_rsrc_t rsrc_t;

namespace {
constexpr int DM = 576, SK = 8192, DH = 128, WIN = 2048, FF = 4608, QD = 4096;
constexpr int AUX_SC1 = 16;
#define GATHER_AUX 16
#define WS_KV 4
#define WS_E 5
#define Q_DELAY 380
#define POLL_SLEEP 2
#define POLL_GAP 8
#define CODE_BYTES 14400
constexpr unsigned SPIN_CAP = 30000;

constexpr int WS_EPOCH = 0;
constexpr int WS_GU = 4096;
constexpr int WS_QP = WS_GU + 144 * 1024;
constexpr int WS_PART = WS_QP + 128 * 2048;
constexpr int WS_STAT = WS_PART + 256 * 1024;
constexpr int WS_FP = WS_STAT + 4096;
constexpr int WS_SP = WS_FP + 72 * 2048;
constexpr int WS_END = WS_SP + 144 * 2048;
constexpr int WS_STAMP = 8 << 20;

constexpr int NSLOT = 18, SLAB = 8192;
constexpr int L_AX = NSLOT * SLAB, AX_STR = 1216;
constexpr int L_PW = L_AX;
constexpr int L_AH = L_AX + 4 * AX_STR, AH_STR = 1216;
constexpr int L_Q = L_AH + 4 * AH_STR;
constexpr int L_PS = L_Q + 512;
constexpr int L_EP = L_PS + 64;
constexpr int L_FLAG = L_EP + 128;
constexpr int F_CONS = L_FLAG, F_TAG = L_FLAG + 16, F_Q = L_FLAG + 20, F_LAND = L_FLAG + 24, F_XN = L_FLAG + 28, F_PART = L_FLAG + 32, F_H = L_FLAG + 36;
constexpr int L_BYTES = L_FLAG + 64;
static_assert(L_BYTES <= 160 * 1024 && L_BYTES > 80 * 1024, "one workgroup per CU");

struct Jobs { unsigned v[512]; };

#define ST(k) do { } while (0)
#define ST_DECL
#define ST_FLUSH() do { } while (0)

__device__ __forceinline__ rsrc_t mk_rsrc(const void* p, int bytes) { return __builtin_amdgcn_make_buffer_rsrc(const_cast<void*>(p), 0, bytes, 0x00020000); }
__device__ __forceinline__ float blo(unsigned u) { return __uint_as_float(u << 16); }
__device__ __forceinline__ float bhi(unsigned u) { return __uint_as_float(u & 0xffff0000u); }
__device__ __forceinline__ unsigned pk2(float lo, float hi) {
  bf16x2 r; r[0] = (__bf16)lo; r[1] = (__bf16)hi; return __builtin_bit_cast(unsigned, r);
}
__device__ __forceinline__ float rbf(float f) { return (float)(__bf16)f; }
template <int CTRL> __device__ __forceinline__ float dpp(float x) {
  return __builtin_bit_cast(float, __builtin_amdgcn_mov_dpp(__builtin_bit_cast(int, x), CTRL, 0xf, 0xf, true));
}
template <int CTRL> __device__ __forceinline__ unsigned dppu(unsigned x) { return (unsigned)__builtin_amdgcn_mov_dpp((int)x, CTRL, 0xf, 0xf, true); }
constexpr int ROR8 = 0x128, ROR4 = 0x124, ROR2 = 0x122, ROR1 = 0x121;
__device__ __forceinline__ float xrow_max(float x) {
  auto s = __builtin_amdgcn_permlane16_swap(__float_as_uint(x), __float_as_uint(x), false, false);
  x = fmaxf(__uint_as_float(s[0]), __uint_as_float(s[1]));
  auto t = __builtin_amdgcn_permlane32_swap(__float_as_uint(x), __float_as_uint(x), false, false);
  return fmaxf(__uint_as_float(t[0]), __uint_as_float(t[1]));
}
__device__ __forceinline__ float xrow_sum(float x) {
  auto s = __builtin_amdgcn_permlane16_swap(__float_as_uint(x), __float_as_uint(x), false, false);
  x = __uint_as_float(s[0]) + __uint_as_float(s[1]);
  auto t = __builtin_amdgcn_permlane32_swap(__float_as_uint(x), __float_as_uint(x), false, false);
  return __uint_as_float(t[0]) + __uint_as_float(t[1]);
}
__device__ __forceinline__ float row_sum(float x) { x += dpp<ROR8>(x); x += dpp<ROR4>(x); x += dpp<ROR2>(x); x += dpp<ROR1>(x); return x; }
__device__ __forceinline__ f32x4 mfma32(v4u a, v4u b, f32x4 c) {
  return __builtin_amdgcn_mfma_f32_16x16x32_bf16(__builtin_bit_cast(bf16x8, a), __builtin_bit_cast(bf16x8, b), c, 0, 0, 0);
}
__device__ __forceinline__ f32x4 mfma16(v2u a, v2u b, f32x4 c) {
  return __builtin_amdgcn_mfma_f32_16x16x16bf16_1k(__builtin_bit_cast(s16x4, a), __builtin_bit_cast(s16x4, b), c, 0, 0, 0);
}
#define TAGOK(v) ((((v).y ^ tag) | ((v).w ^ tag)) == 0u)
#define TOUCH(x) asm volatile("" ::"v"(x))

typedef LAS unsigned char* lds_t;
__device__ __forceinline__ v2u tr_read(lds_t p) { return __builtin_bit_cast(v2u, __builtin_amdgcn_ds_read_tr16_b64_v4i16((LAS s16x4*)p)); }
__device__ __forceinline__ unsigned lds_ld(lds_t p) { return __hip_atomic_load((LAS unsigned*)p, __ATOMIC_RELAXED, __HIP_MEMORY_SCOPE_WORKGROUP); }
__device__ __forceinline__ void lds_st(lds_t p, unsigned v) { __hip_atomic_store((LAS unsigned*)p, v, __ATOMIC_RELAXED, __HIP_MEMORY_SCOPE_WORKGROUP); }
__device__ __forceinline__ void lds_wait_ge(lds_t p, unsigned want) {
  while ((unsigned)__builtin_amdgcn_readfirstlane(lds_ld(p)) < want) __builtin_amdgcn_s_sleep(1);
  asm volatile("" ::: "memory");
}
template <int N> __device__ __forceinline__ void wait_vm() { asm volatile("s_waitcnt vmcnt(%0)" ::"i"(N) : "memory"); }
#define DMA_OPS " offen nt lds\n\t"
__device__ __forceinline__ void dma_wslab(rsrc_t r, int voff, int soff, int step, unsigned ldsoff) {
  int so;
#define NEXT "s_add_u32 m0, m0, 0x400\n\ts_add_u32 %0, %0, %4\n\tbuffer_load_dwordx4 %2, %3, %0" DMA_OPS
  asm volatile("s_mov_b32 m0, %1\n\ts_mov_b32 %0, %5\n\tbuffer_load_dwordx4 %2, %3, %0" DMA_OPS NEXT NEXT NEXT NEXT NEXT NEXT NEXT
               : "=&s"(so) : "s"(ldsoff), "v"(voff), "s"(r), "s"(step), "s"(soff) : "memory", "scc");
#undef NEXT
}
__device__ __forceinline__ void dma_kvslab(rsrc_t rk, rsrc_t rv, const int (&vk)[4], const int (&vv)[4], int soff, unsigned ldsoff) {
#define NEXT(V, R) "s_add_u32 m0, m0, 0x400\n\ts_nop 0\n\tbuffer_load_dwordx4 " V ", " R ", %0" DMA_OPS
  asm volatile("s_mov_b32 m0, %1\n\ts_nop 0\n\tbuffer_load_dwordx4 %4, %2, %0" DMA_OPS NEXT("%5", "%2") NEXT("%6", "%2") NEXT("%7", "%2")
               NEXT("%8", "%3") NEXT("%9", "%3") NEXT("%10", "%3") NEXT("%11", "%3")
               :: "s"(soff), "s"(ldsoff), "s"(rk), "s"(rv), "v"(vk[0]), "v"(vk[1]), "v"(vk[2]), "v"(vk[3]), "v"(vv[0]), "v"(vv[1]), "v"(vv[2]), "v"(vv[3]) : "memory", "scc");
#undef NEXT
}
__device__ __forceinline__ int aperm(int k) { const int g = (k >> 2) & 7; return (k >> 5) * 64 + (g & 3) * 16 + (g >> 2) * 8; }

__global__ void __launch_bounds__(384, 2)
fused_decode(const u16* __restrict__ x, const u16* __restrict__ Kc, const u16* __restrict__ Vc, const u16* __restrict__ wq, const u16* __restrict__ wo,
             const u16* __restrict__ wgate, const u16* __restrict__ wup, const u16* __restrict__ wdn, u16* __restrict__ out, unsigned char* __restrict__ ws,
             const Jobs jobs) {
  __shared__ __attribute__((aligned(16))) unsigned char smem[L_BYTES];
  lds_t L = (lds_t)smem;
  unsigned long long pc0;
  asm volatile("s_getpc_b64 %0" : "=s"(pc0));
  const int lane = threadIdx.x & 63, w = __builtin_amdgcn_readfirstlane(threadIdx.x >> 6), wg = blockIdx.x;
  lds_st(L + L_FLAG + (lane & 15) * 4, 0u);
  const unsigned jx = jobs.v[2 * wg], jy = jobs.v[2 * wg + 1];
  asm volatile("" ::"s"(x), "s"(Kc), "s"(Vc), "s"(wq), "s"(wo), "s"(wgate), "s"(wup), "s"(wdn), "s"(out), "s"(ws), "s"(jx), "s"(jy) : "memory");
  asm volatile("s_waitcnt lgkmcnt(0)" ::: "memory");
  const int pair = jx & 63, nkv = (jx >> 13) & 63, type = (jx >> 21) & 3, qj = (int)(jy & 255) - 1, gj = (int)((jy >> 8) & 255) - 1, tile = jy >> 16;
  const int nq = qj < 0 ? 0 : 5 - (qj & 1), qs0 = (qj & 1) * 5, ne = nq + (gj < 0 ? 0 : 9), nl = type == 0 ? 4 : type == 1 ? 9 : 0;
  const int tk = tile & 15, tcg = tile >> 4;
  const rsrc_t rws = mk_rsrc(ws, WS_END);
  ST_DECL;
  ST(0);
  const int r = lane >> 4, c = lane & 15;
  v4u xv[5] = {};
  unsigned ep = 0;
  if (w == 5) {
    const rsrc_t rx = mk_rsrc(x, 4 * DM * 2);
#pragma unroll
    for (int i = 0; i < 5; ++i) xv[i] = __builtin_amdgcn_raw_buffer_load_b128(rx, (i < 4 || c < 8) ? r * (DM * 2) + (c + 16 * i) * 16 : 0x10000, 0, 0);
    ep = __builtin_amdgcn_raw_buffer_load_b32(rws, WS_EPOCH + (lane & 31) * 4, 0, AUX_SC1);
  } else if (w == 1) {
    ep = __builtin_amdgcn_raw_buffer_load_b32(mk_rsrc((const void*)(pc0 & ~255ull), CODE_BYTES), (wg >> 3) * 512 + lane * 8, 0, 0);
  }
  const unsigned long long t0 = __builtin_amdgcn_s_memrealtime();
  __builtin_amdgcn_s_barrier();
  asm volatile("" ::: "memory");

  if (__builtin_expect(w == 4, 1)) {
    __builtin_amdgcn_s_setprio(3);
    TOUCH(xv[4]); TOUCH(ep);
    const int prow = lane >> 3, pch = (lane & 7) ^ (((prow >> 1) & 3) << 1);
    const unsigned ring = (unsigned)(size_t)L;
    int s = 0, pub = 0;
#define PUB(v) do { if ((v) > pub) { pub = (v); if (lane == 0) lds_st(L + F_LAND, pub); } } while (0)
    {
      const rsrc_t rw = mk_rsrc(wq + (qj >> 1) * 64, qj < 0 ? 0 : 575 * (QD * 2) + 128);
      const int voff = prow * (QD * 2) + pch * 16;
#pragma unroll 1
      for (; s < nq; ++s) dma_wslab(rw, voff, (qs0 + s) * 64 * (QD * 2), 8 * (QD * 2), ring + s * SLAB);
    }
    {
      const rsrc_t rw = mk_rsrc(gj >= 72 ? wup + (gj - 72) * 64 : wgate + gj * 64, gj < 0 ? 0 : 575 * (FF * 2) + 128);
      const int voff = prow * (FF * 2) + pch * 16;
#pragma unroll 1
      for (; s < ne; ++s) {
        wait_vm<8 * (WS_E - 1)>();
        PUB(s - (WS_E - 1));
        dma_wslab(rw, voff, (s - nq) * 64 * (FF * 2), 8 * (FF * 2), ring + s * SLAB);
      }
    }
    ST(1);
    int slot = ne;
#define ADMIT() do { \
      wait_vm<8 * (WS_KV - 1)>(); \
      PUB(s - (WS_KV - 1)); \
      if (s >= NSLOT) { \
        bool drained = false; \
        for (;;) { \
          asm volatile("" ::: "memory"); \
          const v4u cv = *(volatile LAS v4u*)(L + F_CONS); \
          if (__builtin_amdgcn_readfirstlane(min(min(cv.x, cv.y), min(cv.z, cv.w))) > (unsigned)(s - NSLOT)) break; \
          if (!drained) { ST(9); wait_vm<0>(); PUB(s); drained = true; } \
          __builtin_amdgcn_s_sleep(1); \
        } \
      } } while (0)
    {
      const size_t kvbase = ((size_t)pair * SK + (SK - WIN) + ((jx >> 6) & 127) * 16) * DH;
      const rsrc_t rk = mk_rsrc(Kc + kvbase, nkv * 4096), rv = mk_rsrc(Vc + kvbase, nkv * 4096);
      const int kr = r, kc = c;
      int vk[4], vv[4];
#pragma unroll
      for (int p = 0; p < 4; ++p) { vk[p] = p * 1024 + kr * 256 + ((kc ^ (4 * p + kr)) << 4); vv[p] = p * 1024 + kr * 256 + ((kc ^ (((4 * p + kr) & 7) << 1)) << 4); }
#pragma unroll 1
      for (int i = 0; i < nkv; ++i, ++s) {
        ADMIT();
        dma_kvslab(rk, rv, vk, vv, i * 4096, ring + slot * SLAB);
        slot = slot + 1 == NSLOT ? 0 : slot + 1;
      }
    }
    ST(4);
    if (nl) {
      const u16* base = type == 0 ? wo + tk * 256 * DM + tcg * 64 : wdn + (tile & 7) * 576 * DM + (tile >> 3) * 64;
      const rsrc_t rw = mk_rsrc(base, (nl * 64 - 1) * (DM * 2) + 128);
      const int voff = prow * (DM * 2) + pch * 16;
#pragma unroll 1
      for (int i = 0; i < nl; ++i, ++s) {
        ADMIT();
        dma_wslab(rw, voff, i * 64 * (DM * 2), 8 * (DM * 2), ring + slot * SLAB);
        slot = slot + 1 == NSLOT ? 0 : slot + 1;
      }
    }
#undef ADMIT
    ST(5);
    if (WS_KV > 5) { wait_vm<40>(); PUB(s - 5); }
    if (WS_KV > 4) { wait_vm<32>(); PUB(s - 4); }
    if (WS_KV > 3) { wait_vm<24>(); PUB(s - 3); }
    wait_vm<16>(); PUB(s - 2);
    wait_vm<8>(); PUB(s - 1);
    wait_vm<0>(); PUB(s);
#undef PUB
    ST(6);
    ST_FLUSH();
    return;
  }

  if (__builtin_expect(w == 5, 1)) {
    __builtin_amdgcn_s_setprio(3);
    {
      float s = 0.f;
#pragma unroll
      for (int i = 0; i < 5; ++i)
#pragma unroll
        for (int k = 0; k < 4; ++k) s += blo(xv[i][k]) + bhi(xv[i][k]);
      const float mean = row_sum(s) * (1.f / DM);
      float q = 0.f, q4 = 0.f;
#pragma unroll
      for (int i = 0; i < 5; ++i)
#pragma unroll
        for (int k = 0; k < 4; ++k) {
          const float a = blo(xv[i][k]) - mean, b = bhi(xv[i][k]) - mean;
          if (i < 4) q = fmaf(a, a, fmaf(b, b, q)); else q4 = fmaf(a, a, fmaf(b, b, q4));
        }
      q += c < 8 ? q4 : 0.f;
      const float var = rbf(row_sum(q) * (1.f / DM)), mb = rbf(mean);
      const float inv = __builtin_amdgcn_rcpf(rbf(__builtin_amdgcn_sqrtf(rbf(var + 1e-5f))));
      lds_t ax = L + L_AX + r * AX_STR + (c >> 2) * 64 + (c & 1) * 32 + ((c >> 1) & 1) * 8;
#pragma unroll
      for (int i = 0; i < 5; ++i) {
        unsigned o[4];
#pragma unroll
        for (int k = 0; k < 4; ++k) {
          const unsigned d = pk2(blo(xv[i][k]) - mb, bhi(xv[i][k]) - mb);
          o[k] = pk2(blo(d) * inv, bhi(d) * inv);
        }
        if (i < 4 || c < 8) { *(LAS v2u*)(ax + i * 256) = v2u{o[0], o[1]}; *(LAS v2u*)(ax + i * 256 + 16) = v2u{o[2], o[3]}; }
      }
    }
    unsigned tag = ep;
    tag += dppu<ROR8>(tag); tag += dppu<ROR4>(tag); tag += dppu<ROR2>(tag); tag += dppu<ROR1>(tag);
    tag = (unsigned)__builtin_amdgcn_readlane(tag, 0) + (unsigned)__builtin_amdgcn_readlane(tag, 16) + 0x9E3779B9u;
    if (lane < 32) lds_st(L + L_EP + lane * 4, ep);
    if (lane == 0) lds_st(L + F_TAG, tag);
    asm volatile("s_waitcnt lgkmcnt(0)" ::: "memory");
    if (lane == 0) lds_st(L + F_XN, 1u);
    ST(1);
    while (__builtin_amdgcn_s_memrealtime() - t0 < Q_DELAY) __builtin_amdgcn_s_sleep(2);
    ST(2);
    {
      int o[4];
      v4u g[4];
#pragma unroll
      for (int k = 0; k < 4; ++k) { o[k] = WS_QP + ((pair & 15) * 4 + r) * 4096 + (k >> 1) * 2048 + (pair >> 4) * 512 + c * 32 + (k & 1) * 16; g[k] = v4u{0u, 0u, 0u, 0u}; }
      for (unsigned spin = 0; spin < SPIN_CAP; ++spin) {
        asm volatile("" ::: "memory");
        v4u a[4];
#pragma unroll
        for (int k = 0; k < 4; ++k) a[k] = __builtin_amdgcn_raw_buffer_load_b128(rws, o[k], 0, GATHER_AUX);
        bool done = true;
#pragma unroll
        for (int k = 0; k < 4; ++k) { const bool ok = TAGOK(a[k]); g[k] = ok ? a[k] : g[k]; o[k] = ok ? WS_END : o[k]; done = done & (o[k] == WS_END); }
        if (__all(done)) break;
      }
      *(LAS v2u*)(L + L_Q + lane * 8) = v2u{pk2(__uint_as_float(g[0].x) + __uint_as_float(g[2].x), __uint_as_float(g[0].z) + __uint_as_float(g[2].z)),
                                            pk2(__uint_as_float(g[1].x) + __uint_as_float(g[3].x), __uint_as_float(g[1].z) + __uint_as_float(g[3].z))};
      asm volatile("s_waitcnt lgkmcnt(0)" ::: "memory");
      if (lane == 0) lds_st(L + F_Q, 1u);
    }
    ST(3);
    if (type == 1) {
      const int off = WS_GU + (tile & 7) * 9 * 1024 + lane * 16;
      lds_t ah = L + L_AH + (c >> 2) * AH_STR + aperm(r * 16 + (c & 3) * 4);
      v4u g[3], u[3], gn[3], un[3];
#pragma unroll
      for (int e = 0; e < 3; ++e) { gn[e] = __builtin_amdgcn_raw_buffer_load_b128(rws, off, e * 1024, GATHER_AUX); un[e] = __builtin_amdgcn_raw_buffer_load_b128(rws, off, (72 + e) * 1024, GATHER_AUX); }
      unsigned spin = 0;
#pragma unroll 1
      for (int j = 0; j < 9;) {
#pragma unroll
        for (int e = 0; e < 3; ++e) { g[e] = gn[e]; u[e] = un[e]; }
        const bool ok = __all(TAGOK(g[0]) & TAGOK(u[0]) & TAGOK(g[1]) & TAGOK(u[1]) & TAGOK(g[2]) & TAGOK(u[2])) || ++spin >= SPIN_CAP;
        const int jn = ok ? j + 3 : j;
        asm volatile("" ::: "memory");
        if (jn < 9) {
#pragma unroll
          for (int e = 0; e < 3; ++e) { gn[e] = __builtin_amdgcn_raw_buffer_load_b128(rws, off + jn * 1024, e * 1024, GATHER_AUX); un[e] = __builtin_amdgcn_raw_buffer_load_b128(rws, off + jn * 1024, (72 + e) * 1024, GATHER_AUX); }
        }
        if (ok) {
#pragma unroll
          for (int e = 0; e < 3; ++e) {
            const float gv[4] = {blo(g[e].x), bhi(g[e].x), blo(g[e].z), bhi(g[e].z)}, uv[4] = {blo(u[e].x), bhi(u[e].x), blo(u[e].z), bhi(u[e].z)};
            float hv[4];
#pragma unroll
            for (int i = 0; i < 4; ++i) {
              const float a = gv[i], inner = 0.7978845608028654f * fmaf(0.044715f * a * a, a, a);
              hv[i] = rbf(a * __builtin_amdgcn_rcpf(1.f + __builtin_amdgcn_exp2f(-2.8853900817779268f * inner))) * uv[i];
            }
            *(LAS v2u*)(ah + (j + e) * 128) = v2u{pk2(hv[0], hv[1]), pk2(hv[2], hv[3])};
          }
        }
        j = jn;
      }
      asm volatile("s_waitcnt lgkmcnt(0)" ::: "memory");
      if (lane == 0) lds_st(L + F_H, 1u);
    }
    ST(4);
    ST_FLUSH();
    return;
  }

  unsigned xres = 0;
  const int ob = tk >> 2, ocol = tcg * 64 + (tk & 3) * 16 + (lane & 7) * 2;
  if (w == 0 && type == 0) xres = *(const unsigned*)(x + ob * DM + ocol);
  const int q4 = (lane >> 2) & 3, p4 = lane & 3;
  const int wtro = (4 * r + q4) * 128 + (((2 * w + (p4 >> 1)) ^ (((2 * r + (q4 >> 1)) & 3) << 1)) << 4) + (p4 & 1) * 8;
#define GEMV_SLAB(slotoff, ap, acc) do { \
    const v2u l0_ = tr_read(L + (slotoff) + wtro), h0_ = tr_read(L + (slotoff) + wtro + 2048), l1_ = tr_read(L + (slotoff) + wtro + 4096), h1_ = tr_read(L + (slotoff) + wtro + 6144); \
    const v4u b0_ = *(LAS v4u*)(ap), b1_ = *(LAS v4u*)((ap) + 64); \
    acc = mfma32(v4u{l0_.x, l0_.y, h0_.x, h0_.y}, b0_, acc); acc = mfma32(v4u{l1_.x, l1_.y, h1_.x, h1_.y}, b1_, acc); } while (0)
  lds_wait_ge(L + F_XN, 1);
  TOUCH(ep);
  const unsigned tag = lds_ld(L + F_TAG);
  ST(1);
  const int x4 = c >> 2, f32off = x4 < 2 ? (c & 3) * 512 + (8 * w + 2 * r + x4) * 16 : WS_END;
#define PUBLISH_F32(dst, y) __builtin_amdgcn_raw_buffer_store_b128(v4u{__float_as_uint((x4 & 1) ? y[2] : y[0]), tag, __float_as_uint((x4 & 1) ? y[3] : y[1]), tag}, rws, f32off, (dst), AUX_SC1)
  if (nq) {
    f32x4 y = {0.f, 0.f, 0.f, 0.f};
    lds_t ap = L + L_AX + p4 * AX_STR + r * 16 + qs0 * 128;
#pragma unroll 1
    for (int j = 0; j < nq; ++j) {
      lds_wait_ge(L + F_LAND, j + 1);
      GEMV_SLAB(j * SLAB, ap + j * 128, y);
    }
    PUBLISH_F32(WS_QP + qj * 2048, y);
  }
  ST(4);
  if (gj >= 0) {
    f32x4 y = {0.f, 0.f, 0.f, 0.f};
    lds_t ap = L + L_AX + p4 * AX_STR + r * 16;
#pragma unroll 1
    for (int j = 0; j < 9; ++j) {
      lds_wait_ge(L + F_LAND, nq + j + 1);
      GEMV_SLAB((nq + j) * SLAB, ap + j * 128, y);
    }
    __builtin_amdgcn_raw_buffer_store_b128(v4u{pk2(y[0], y[1]), tag, pk2(y[2], y[3]), tag}, rws, c < 4 ? WS_GU + gj * 1024 + w * 256 + c * 64 + r * 16 : WS_END, 0, AUX_SC1);
  }
  if (lane == 0) lds_st(L + F_CONS + w * 4, ne + w);
  ST(2);
  lds_wait_ge(L + F_Q, 1);
  ST(3);
  v4u qb[4];
#pragma unroll
  for (int s = 0; s < 4; ++s) qb[s] = *(LAS v4u*)(L + L_Q + (c & 1) * 256 + s * 64 + r * 16);
  int kro[4], vro[8];
#pragma unroll
  for (int s = 0; s < 4; ++s) kro[s] = c * 256 + (((4 * s + r) ^ c) << 4);
#pragma unroll
  for (int t = 0; t < 8; ++t) vro[t] = 4096 + (4 * r + q4) * 256 + (((2 * t + (p4 >> 1)) ^ (((4 * r + q4) & 7) << 1)) << 4) + (p4 & 1) * 8;
  constexpr float C2 = 0.08838834764831845f * 1.4426950408889634f;
  float m = -INFINITY, l = 0.f;
  f32x4 acc[8];
#pragma unroll
  for (int t = 0; t < 8; ++t) acc[t] = f32x4{0.f, 0.f, 0.f, 0.f};
  {
    int slot = ne + w;
#pragma unroll 1
    for (int i = w; i < nkv; i += 4) {
      lds_wait_ge(L + F_LAND, ne + i + 1);
      lds_t sb = L + slot * SLAB;
      v4u kf[4]; v2u vf[8];
#pragma unroll
      for (int s = 0; s < 4; ++s) kf[s] = *(LAS v4u*)(sb + kro[s]);
#pragma unroll
      for (int t = 0; t < 8; ++t) vf[t] = tr_read(sb + vro[t]);
      const f32x4 zero = {0.f, 0.f, 0.f, 0.f};
      const f32x4 z = (mfma32(kf[1], qb[1], mfma32(kf[0], qb[0], zero)) + mfma32(kf[3], qb[3], mfma32(kf[2], qb[2], zero))) * C2;
      const float mx = xrow_max(fmaxf(fmaxf(z[0], z[1]), fmaxf(z[2], z[3])));
      if (__any(mx > m)) {
        const float mn = fmaxf(m, mx), al = __builtin_amdgcn_exp2f(m - mn);
        m = mn; l *= al;
#pragma unroll
        for (int t = 0; t < 8; ++t) acc[t] *= al;
      }
      const v2u P = {pk2(__builtin_amdgcn_exp2f(z[0] - m), __builtin_amdgcn_exp2f(z[1] - m)), pk2(__builtin_amdgcn_exp2f(z[2] - m), __builtin_amdgcn_exp2f(z[3] - m))};
      l += (blo(P.x) + bhi(P.x)) + (blo(P.y) + bhi(P.y));
#pragma unroll
      for (int t = 0; t < 8; ++t) acc[t] = mfma16(vf[t], P, acc[t]);
      asm volatile("s_waitcnt lgkmcnt(0)" ::: "memory");
      if (lane == 0) lds_st(L + F_CONS + w * 4, ne + min(i + 4, nkv));
      slot = slot + 4 >= NSLOT ? slot + 4 - NSLOT : slot + 4;
    }
  }
  l = xrow_sum(l);
  ST(5);
  if (c < 2) {
#pragma unroll
    for (int t = 0; t < 8; ++t) *(LAS f32x4*)(L + L_PW + w * 1024 + c * 512 + t * 64 + r * 16) = acc[t];
    if (r == 0) { *(LAS float*)(L + L_PS + w * 16 + c * 4) = m; *(LAS float*)(L + L_PS + w * 16 + 8 + c * 4) = l; }
  }
  asm volatile("s_waitcnt lgkmcnt(0)" ::: "memory");
  unsigned arrived = 0;
  if (lane == 0) arrived = __hip_atomic_fetch_add((LAS unsigned*)(L + F_PART), 1u, __ATOMIC_RELAXED, __HIP_MEMORY_SCOPE_WORKGROUP);
  arrived = __builtin_amdgcn_readfirstlane(arrived);
  asm volatile("" ::: "memory");
  const int hd = lane >> 5;
  if (arrived == 3) {
    float mw[4], lw[4];
#pragma unroll
    for (int k = 0; k < 4; ++k) { mw[k] = *(LAS float*)(L + L_PS + k * 16 + hd * 4); lw[k] = *(LAS float*)(L + L_PS + k * 16 + 8 + hd * 4); }
    const float M = fmaxf(fmaxf(mw[0], mw[1]), fmaxf(mw[2], mw[3]));
    float Ls = 0.f; f32x4 o = {0.f, 0.f, 0.f, 0.f};
#pragma unroll
    for (int k = 0; k < 4; ++k) { const float e = __builtin_amdgcn_exp2f(mw[k] - M); Ls = fmaf(e, lw[k], Ls); o += e * *(LAS f32x4*)(L + L_PW + k * 1024 + lane * 16); }
    const float inv = __builtin_amdgcn_rcpf(Ls), lm = M + __builtin_amdgcn_logf(Ls);
    const unsigned lam0 = __builtin_amdgcn_readlane(__float_as_uint(lm), 0), lam1 = __builtin_amdgcn_readlane(__float_as_uint(lm), 32);
    const int me = (pair & 15) * 16 + (pair >> 4) * 4 + ((jx >> 19) & 3);
    __builtin_amdgcn_raw_buffer_store_b128(v4u{pk2(o[0] * inv, o[1] * inv), tag, pk2(o[2] * inv, o[3] * inv), tag}, rws, WS_PART + me * 1024 + lane * 16, 0, AUX_SC1);
    if (lane == 0) __builtin_amdgcn_raw_buffer_store_b128(v4u{lam0, tag, lam1, tag}, rws, WS_STAT + me * 16, 0, AUX_SC1);
  }
  ST(6);
  if (type == 2) { ST_FLUSH(); return; }

  const int lslot = ne + nkv - (ne + nkv >= 2 * NSLOT ? 2 * NSLOT : NSLOT);
  if (type == 0) {
    v4u d[4] = {}, sv = {};
    const int p0 = tk * 16 + w * 4;
    unsigned miss = 31u;
    for (unsigned spin = 0; spin < SPIN_CAP; ++spin) {
      asm volatile("" ::: "memory");
#pragma unroll
      for (int k = 0; k < 4; ++k) if (miss & (1u << k)) d[k] = __builtin_amdgcn_raw_buffer_load_b128(rws, WS_PART + p0 * 1024 + lane * 16, k * 1024, GATHER_AUX);
      if (miss & 16u) sv = __builtin_amdgcn_raw_buffer_load_b128(rws, WS_STAT + (p0 + (lane & 3)) * 16, 0, GATHER_AUX);
#pragma unroll
      for (int k = 0; k < 4; ++k) if (__all(TAGOK(d[k]))) miss &= ~(1u << k);
      if (__all(TAGOK(sv))) miss &= ~16u;
      if (!miss) break;
      __builtin_amdgcn_s_sleep(POLL_SLEEP);
    }
    ST(7);
    const float lm = __uint_as_float(hd ? sv.z : sv.x);
    const float la[4] = {dpp<0x00>(lm), dpp<0x55>(lm), dpp<0xAA>(lm), dpp<0xFF>(lm)};
    const float M = fmaxf(fmaxf(la[0], la[1]), fmaxf(la[2], la[3]));
    float e[4], Ls = 0.f;
#pragma unroll
    for (int k = 0; k < 4; ++k) { e[k] = __builtin_amdgcn_exp2f(la[k] - M); Ls += e[k]; }
    const float inv = __builtin_amdgcn_rcpf(Ls);
    f32x4 o = {0.f, 0.f, 0.f, 0.f};
#pragma unroll
    for (int k = 0; k < 4; ++k) o += e[k] * f32x4{blo(d[k].x), bhi(d[k].x), blo(d[k].z), bhi(d[k].z)};
    *(LAS v2u*)(L + L_AH + w * AH_STR + aperm(lane * 4)) = v2u{pk2(o[0] * inv, o[1] * inv), pk2(o[2] * inv, o[3] * inv)};
    lds_wait_ge(L + F_LAND, ne + nkv + 4);
    v2u wf[16];
#pragma unroll
    for (int j = 0; j < 4; ++j) {
      const int so = (lslot + j >= NSLOT ? lslot + j - NSLOT : lslot + j) * SLAB + wtro;
#pragma unroll
      for (int k = 0; k < 4; ++k) wf[4 * j + k] = tr_read(L + so + k * 2048);
    }
    asm volatile("s_waitcnt lgkmcnt(0)" ::: "memory");
    __builtin_amdgcn_s_barrier();
    asm volatile("" ::: "memory");
    lds_t ap = L + L_AH + p4 * AH_STR + r * 16;
    f32x4 y = {0.f, 0.f, 0.f, 0.f}, y2 = {0.f, 0.f, 0.f, 0.f};
#pragma unroll
    for (int j = 0; j < 4; ++j) {
      y = mfma32(v4u{wf[4 * j].x, wf[4 * j].y, wf[4 * j + 1].x, wf[4 * j + 1].y}, *(LAS v4u*)(ap + j * 128), y);
      y2 = mfma32(v4u{wf[4 * j + 2].x, wf[4 * j + 2].y, wf[4 * j + 3].x, wf[4 * j + 3].y}, *(LAS v4u*)(ap + j * 128 + 64), y2);
    }
    y += y2;
    PUBLISH_F32(WS_SP + tile * 2048, y);
  } else {
    lds_wait_ge(L + F_H, 1);
    f32x4 y = {0.f, 0.f, 0.f, 0.f};
    lds_t ap = L + L_AH + p4 * AH_STR + r * 16;
    int slot = lslot;
#pragma unroll 1
    for (int j = 0; j < 9; ++j) {
      lds_wait_ge(L + F_LAND, ne + nkv + j + 1);
      GEMV_SLAB(slot * SLAB, ap + j * 128, y);
      slot = slot + 1 == NSLOT ? 0 : slot + 1;
    }
    PUBLISH_F32(WS_FP + tile * 2048, y);
  }
#undef PUBLISH_F32
#undef GEMV_SLAB
  ST(8);
  if (type == 0 && w == 0) {
    const int p = lane >> 3, lo16 = ob * 512 + (tk & 3) * 128 + (lane & 7) * 16;
    v4u s0, s1, f0;
    for (unsigned spin = 0; spin < SPIN_CAP; ++spin) {
      asm volatile("" ::: "memory");
      s0 = __builtin_amdgcn_raw_buffer_load_b128(rws, WS_SP + (tcg * 16 + p) * 2048 + lo16, 0, GATHER_AUX);
      s1 = __builtin_amdgcn_raw_buffer_load_b128(rws, WS_SP + (tcg * 16 + 8 + p) * 2048 + lo16, 0, GATHER_AUX);
      f0 = __builtin_amdgcn_raw_buffer_load_b128(rws, WS_FP + (tcg * 8 + p) * 2048 + lo16, 0, GATHER_AUX);
      if (__all(TAGOK(s0) & TAGOK(s1) & TAGOK(f0))) break;
    }
    float sa = __uint_as_float(s0.x) + __uint_as_float(s1.x), sb = __uint_as_float(s0.z) + __uint_as_float(s1.z);
    float fa = __uint_as_float(f0.x), fb = __uint_as_float(f0.z);
    sa += dpp<ROR8>(sa); sb += dpp<ROR8>(sb); fa += dpp<ROR8>(fa); fb += dpp<ROR8>(fb);
    sa = rbf(xrow_sum(sa)); sb = rbf(xrow_sum(sb)); fa = rbf(xrow_sum(fa)); fb = rbf(xrow_sum(fb));
    const float ga = rbf(fa * __builtin_amdgcn_rcpf(1.f + __builtin_amdgcn_exp2f(-1.4426950408889634f * fa))), gb = rbf(fb * __builtin_amdgcn_rcpf(1.f + __builtin_amdgcn_exp2f(-1.4426950408889634f * fb)));
    const unsigned res = pk2(blo(xres) + rbf(sa * ga), bhi(xres) + rbf(sb * gb));
    if (lane < 8) *(unsigned*)(out + ob * DM + ocol) = res;
    if (tile == 0 && lane < 32) __builtin_amdgcn_raw_buffer_store_b32(lds_ld(L + L_EP + lane * 4) + 1u, rws, WS_EPOCH + lane * 4, 0, AUX_SC1);
  }
  ST(9);
  ST_FLUSH();
}

#define PEN3 50
#define ODDPEN 10
static void build_jobs(Jobs& J, int* kind_out = nullptr) {
  int wgs[3][256], nw[3] = {0, 0, 0};
  int type[256], tile[256], qj[256], gj[256], cost[256];
  for (int wg = 0; wg < 256; ++wg) {
    const int xcd = wg & 7, idx = wg >> 3, gi = idx & 15, o = (idx >> 4) * 7 + gi - 9;
    type[wg] = gi < 9 ? 0 : o < 9 ? 1 : 2;
    tile[wg] = gi < 9 ? gi * 16 + xcd * 2 + (idx >> 4) : o < 9 ? o * 8 + xcd : 0;
    wgs[type[wg]][nw[type[wg]]++] = wg;
    qj[wg] = gj[wg] = -1;
  }
  int fast[126], nf = 0, slow[18], nsl = 0, halves[128], nh = 0;
  for (int g = 0; g < 144; ++g) { if ((g & 7) == 3) slow[nsl++] = g; else fast[nf++] = g; }
  for (int h = 0; h < 128; ++h) if (((h >> 1) & 7) == 3) halves[nh++] = h;
  for (int h = 0; h < 128; ++h) if (((h >> 1) & 7) != 3 && !(h & 1)) halves[nh++] = h;
  for (int h = 0; h < 128; ++h) if (((h >> 1) & 7) != 3 && (h & 1)) halves[nh++] = h;
  int ih = 0;
  for (int i = 0; i < 40; ++i) { if (i < 22) qj[wgs[2][i]] = halves[ih++]; else gj[wgs[2][i]] = slow[i - 22]; }
  for (int i = 0; i < 72; ++i) qj[wgs[1][i]] = halves[ih++];
  for (int i = 0; i < 144; ++i) { if (i < 34) qj[wgs[0][i]] = halves[ih++]; if (i >= 18) gj[wgs[0][i]] = fast[i - 18]; }
  for (int wg = 0; wg < 256; ++wg) {
    const int nq = qj[wg] < 0 ? 0 : 5 - (qj[wg] & 1);
    cost[wg] = 10 * nq + (nq && ((qj[wg] >> 1) & 7) == 3 ? PEN3 * nq / 9 : 0) + (gj[wg] < 0 ? 0 : (gj[wg] & 7) == 3 ? 90 + PEN3 : 90) +
               (type[wg] == 1 ? 50 : 0) + ((wg & 1) ? ODDPEN : 0);
  }
  int ord[256];
  for (int i = 0; i < 256; ++i) ord[i] = i;
  for (int i = 1; i < 256; ++i) { const int v = ord[i]; int j = i; while (j > 0 && cost[ord[j - 1]] < cost[v]) { ord[j] = ord[j - 1]; --j; } ord[j] = v; }
  for (int p = 0; p < 64; ++p) {
    const int mem[4] = {ord[p], ord[127 - p], ord[128 + p], ord[255 - p]};
    const int T = (1280 + cost[mem[0]] + cost[mem[1]] + cost[mem[2]] + cost[mem[3]]) / 4;
    int n[4], sum = 0;
    for (int j = 0; j < 4; ++j) { n[j] = (T - cost[mem[j]] + 5) / 10; sum += n[j]; }
    for (int j = 3; sum != 128; j = (j + 3) & 3) { if (sum < 128) { ++n[j]; ++sum; } else { --n[3 - j]; --sum; } }
    int s0 = 0;
    for (int j = 0; j < 4; ++j) {
      const int wg = mem[j];
      J.v[2 * wg] = (unsigned)p | (unsigned)s0 << 6 | (unsigned)n[j] << 13 | (unsigned)j << 19 | (unsigned)type[wg] << 21;
      J.v[2 * wg + 1] = (unsigned)(qj[wg] + 1) | (unsigned)(gj[wg] + 1) << 8 | (unsigned)tile[wg] << 16;
      s0 += n[j];
    }
  }
  if (kind_out) for (int wg = 0; wg < 256; ++wg) kind_out[wg] = type[wg] == 0 ? (qj[wg] < 0 ? 0 : gj[wg] < 0 ? 5 : 2) : type[wg] == 1 ? 1 : gj[wg] >= 0 ? 3 : 4;
}
}

extern "C" void kernel_launch(void* const* d_in, const int* in_sizes, int n_in, void* d_out, int out_size, void* d_ws, size_t ws_size, hipStream_t stream) {
  Jobs J;
  build_jobs(J);
  fused_decode<<<256, 384, 0, stream>>>((const u16*)d_in[0], (const u16*)d_in[1], (const u16*)d_in[2], (const u16*)d_in[3], (const u16*)d_in[4],
                                        (const u16*)d_in[6], (const u16*)d_in[7], (const u16*)d_in[8], (u16*)d_out, (unsigned char*)d_ws, J);
}
```

```cpp
#include <hip/hip_runtime.h>
#include <hip/hip_bf16.h>
#include <stdint.h>

#define LAS __attribute__((address_space(3)))
typedef unsigned v4u __attribute__((ext_vector_type(4)));
typedef unsigned v2u __attribute__((ext_vector_type(2)));
typedef float f32x4 __attribute__((ext_vector_type(4)));
typedef short s16x4 __attribute__((ext_vector_type(4)));
typedef __bf16 bf16x2 __attribute__((ext_vector_type(2)));
typedef __bf16 bf16x8 __attribute__((ext_vector_type(8)));
typedef unsigned short u16;
typedef __amdgpu_buffer_rsrc_t rsrc_t;

namespace {
constexpr int DM = 576, SK = 8192, DH = 128, WIN = 2048, FF = 4608, QD = 4096;
constexpr int AUX_SC1 = 16;
#define GATHER_AUX 16
#define WS_KV 4
#define WS_E 5
#define WS_L 4
#define Q_DELAY 370
#define POLL_SLEEP 2
#define POLL_GAP 8
#define SPIN_SLEEP 0
#define PUB_LANE0 0
#define HALF_PUB 1
#define EARLY_GATHER 0
#define CODE_BYTES 14150
constexpr unsigned SPIN_CAP = 30000;

constexpr int WS_EPOCH = 0;
constexpr int WS_GU = 4096;
constexpr int WS_QP = WS_GU + 144 * 1024;
constexpr int WS_PART = WS_QP + 128 * 2048;
constexpr int WS_STAT = WS_PART + 256 * 1024;
constexpr int WS_FP = WS_STAT + 4096;
constexpr int WS_SP = WS_FP + 72 * 2048;
constexpr int WS_END = WS_SP + 144 * 2048;
constexpr int WS_STAMP = 8 << 20;

constexpr int NSLOT = 18, SLAB = 8192;
constexpr int L_AX = NSLOT * SLAB, AX_STR = 1216;
constexpr int L_PW = L_AX;
constexpr int L_AH = L_AX + 4 * AX_STR, AH_STR = 1216;
constexpr int L_Q = L_AH + 4 * AH_STR;
constexpr int L_PS = L_Q + 512;
constexpr int L_EP = L_PS + 64;
constexpr int L_FLAG = L_EP + 128;
constexpr int F_CONS = L_FLAG, F_TAG = L_FLAG + 16, F_Q = L_FLAG + 20, F_LAND = L_FLAG + 24, F_XN = L_FLAG + 28, F_PART = L_FLAG + 32, F_H = L_FLAG + 36;
constexpr int L_BYTES = L_FLAG + 64;
static_assert(L_BYTES <= 160 * 1024 && L_BYTES > 80 * 1024, "one workgroup per CU");

struct Jobs { unsigned v[512]; };

#define ST(k) do { } while (0)
#define ST_DECL
#define ST_FLUSH() do { } while (0)

__device__ __forceinline__ rsrc_t mk_rsrc(const void* p, int bytes) { return __builtin_amdgcn_make_buffer_rsrc(const_cast<void*>(p), 0, bytes, 0x00020000); }
__device__ __forceinline__ float blo(unsigned u) { return __uint_as_float(u << 16); }
__device__ __forceinline__ float bhi(unsigned u) { return __uint_as_float(u & 0xffff0000u); }
__device__ __forceinline__ unsigned pk2(float lo, float hi) {
  bf16x2 r; r[0] = (__bf16)lo; r[1] = (__bf16)hi; return __builtin_bit_cast(unsigned, r);
}
__device__ __forceinline__ float rbf(float f) { return (float)(__bf16)f; }
template <int CTRL> __device__ __forceinline__ float dpp(float x) {
  return __builtin_bit_cast(float, __builtin_amdgcn_mov_dpp(__builtin_bit_cast(int, x), CTRL, 0xf, 0xf, true));
}
template <int CTRL> __device__ __forceinline__ unsigned dppu(unsigned x) { return (unsigned)__builtin_amdgcn_mov_dpp((int)x, CTRL, 0xf, 0xf, true); }
constexpr int ROR8 = 0x128, ROR4 = 0x124, ROR2 = 0x122, ROR1 = 0x121;
__device__ __forceinline__ float xrow_max(float x) {
  auto s = __builtin_amdgcn_permlane16_swap(__float_as_uint(x), __float_as_uint(x), false, false);
  x = fmaxf(__uint_as_float(s[0]), __uint_as_float(s[1]));
  auto t = __builtin_amdgcn_permlane32_swap(__float_as_uint(x), __float_as_uint(x), false, false);
  return fmaxf(__uint_as_float(t[0]), __uint_as_float(t[1]));
}
__device__ __forceinline__ float xrow_sum(float x) {
  auto s = __builtin_amdgcn_permlane16_swap(__float_as_uint(x), __float_as_uint(x), false, false);
  x = __uint_as_float(s[0]) + __uint_as_float(s[1]);
  auto t = __builtin_amdgcn_permlane32_swap(__float_as_uint(x), __float_as_uint(x), false, false);
  return __uint_as_float(t[0]) + __uint_as_float(t[1]);
}
__device__ __forceinline__ float row_sum(float x) { x += dpp<ROR8>(x); x += dpp<ROR4>(x); x += dpp<ROR2>(x); x += dpp<ROR1>(x); return x; }
__device__ __forceinline__ f32x4 mfma32(v4u a, v4u b, f32x4 c) {
  return __builtin_amdgcn_mfma_f32_16x16x32_bf16(__builtin_bit_cast(bf16x8, a), __builtin_bit_cast(bf16x8, b), c, 0, 0, 0);
}
__device__ __forceinline__ f32x4 mfma16(v2u a, v2u b, f32x4 c) {
  return __builtin_amdgcn_mfma_f32_16x16x16bf16_1k(__builtin_bit_cast(s16x4, a), __builtin_bit_cast(s16x4, b), c, 0, 0, 0);
}
#define TAGX(v) (((v).y ^ tag) | ((v).w ^ tag))
#define TAGOK(v) (TAGX(v) == 0u)
#define BADLANES(v) (__builtin_amdgcn_ballot_w64((v).y != tag) | __builtin_amdgcn_ballot_w64((v).w != tag))
#define TOUCH(x) asm volatile("" ::"v"(x))

typedef LAS unsigned char* lds_t;
__device__ __forceinline__ v2u tr_read(lds_t p) { return __builtin_bit_cast(v2u, __builtin_amdgcn_ds_read_tr16_b64_v4i16((LAS s16x4*)p)); }
__device__ __forceinline__ unsigned lds_ld(lds_t p) { return __hip_atomic_load((LAS unsigned*)p, __ATOMIC_RELAXED, __HIP_MEMORY_SCOPE_WORKGROUP); }
__device__ __forceinline__ void lds_st(lds_t p, unsigned v) { __hip_atomic_store((LAS unsigned*)p, v, __ATOMIC_RELAXED, __HIP_MEMORY_SCOPE_WORKGROUP); }
__device__ __forceinline__ void lds_wait_ge(lds_t p, unsigned want) {
  while ((unsigned)__builtin_amdgcn_readfirstlane(lds_ld(p)) < want) __builtin_amdgcn_s_sleep(1);
  asm volatile("" ::: "memory");
}
__device__ __forceinline__ void lds_spin_ge(lds_t p, unsigned want) {
  unsigned v;
  do v = __builtin_amdgcn_readfirstlane(lds_ld(p)); while (v < want);
  asm volatile("" ::: "memory");
}
template <int N> __device__ __forceinline__ void wait_vm() { asm volatile("s_waitcnt vmcnt(%0)" ::"i"(N) : "memory"); }
#define DMA_OPS " offen nt lds\n\t"
__device__ __forceinline__ void dma_wslab(rsrc_t r, int voff, int soff, int step, unsigned ldsoff) {
  int so;
#define NEXT "s_add_u32 m0, m0, 0x400\n\ts_add_u32 %0, %0, %4\n\tbuffer_load_dwordx4 %2, %3, %0" DMA_OPS
  asm volatile("s_mov_b32 m0, %1\n\ts_mov_b32 %0, %5\n\tbuffer_load_dwordx4 %2, %3, %0" DMA_OPS NEXT NEXT NEXT NEXT NEXT NEXT NEXT
               : "=&s"(so) : "s"(ldsoff), "v"(voff), "s"(r), "s"(step), "s"(soff) : "memory", "scc");
#undef NEXT
}
__device__ __forceinline__ void dma_kvslab(rsrc_t rk, rsrc_t rv, const int (&vk)[4], const int (&vv)[4], int soff, unsigned ldsoff) {
#define NEXT(V, R) "s_add_u32 m0, m0, 0x400\n\ts_nop 0\n\tbuffer_load_dwordx4 " V ", " R ", %0" DMA_OPS
  asm volatile("s_mov_b32 m0, %1\n\ts_nop 0\n\tbuffer_load_dwordx4 %4, %2, %0" DMA_OPS NEXT("%5", "%2") NEXT("%6", "%2") NEXT("%7", "%2")
               NEXT("%8", "%3") NEXT("%9", "%3") NEXT("%10", "%3") NEXT("%11", "%3")
               :: "s"(soff), "s"(ldsoff), "s"(rk), "s"(rv), "v"(vk[0]), "v"(vk[1]), "v"(vk[2]), "v"(vk[3]), "v"(vv[0]), "v"(vv[1]), "v"(vv[2]), "v"(vv[3]) : "memory", "scc");
#undef NEXT
}
__device__ __forceinline__ int aperm(int k) { const int g = (k >> 2) & 7; return (k >> 5) * 64 + (g & 3) * 16 + (g >> 2) * 8; }

__global__ void __launch_bounds__(384, 2)
fused_decode(const u16* __restrict__ x, const u16* __restrict__ Kc, const u16* __restrict__ Vc, const u16* __restrict__ wq, const u16* __restrict__ wo,
             const u16* __restrict__ wgate, const u16* __restrict__ wup, const u16* __restrict__ wdn, u16* __restrict__ out, unsigned char* __restrict__ ws,
             const Jobs jobs) {
  __shared__ __attribute__((aligned(16))) unsigned char smem[L_BYTES];
  lds_t L = (lds_t)smem;
  unsigned long long pc0;
  asm volatile("s_getpc_b64 %0" : "=s"(pc0));
  const int lane = threadIdx.x & 63, w = __builtin_amdgcn_readfirstlane(threadIdx.x >> 6), wg = blockIdx.x;
  lds_st(L + L_FLAG + (lane & 15) * 4, 0u);
  const unsigned jx = jobs.v[2 * wg], jy = jobs.v[2 * wg + 1];
  asm volatile("" ::"s"(x), "s"(Kc), "s"(Vc), "s"(wq), "s"(wo), "s"(wgate), "s"(wup), "s"(wdn), "s"(out), "s"(ws), "s"(jx), "s"(jy) : "memory");
  asm volatile("s_waitcnt lgkmcnt(0)" ::: "memory");
  const int pair = jx & 63, nkv = (jx >> 13) & 63, type = (jx >> 21) & 3, qj = (int)(jy & 255) - 1, gj = (int)((jy >> 8) & 255) - 1, tile = jy >> 16;
  const int nq = qj < 0 ? 0 : 5 - (qj & 1), qs0 = (qj & 1) * 5, ne = nq + (gj < 0 ? 0 : 9), nl = type == 0 ? 4 : type == 1 ? 9 : 0;
  const int tk = tile & 15, tcg = tile >> 4;
  const rsrc_t rws = mk_rsrc(ws, WS_END);
  ST_DECL;
  ST(0);
  const int r = lane >> 4, c = lane & 15;
  if (w == 5) {
    const rsrc_t rx = mk_rsrc(x, 4 * DM * 2);
    const int xo = r * (DM * 2) + c * 16;
    asm volatile("buffer_load_dwordx4 v[220:223], %0, %3, 0 offen\n\tbuffer_load_dwordx4 v[224:227], %0, %3, 0 offen offset:256\n\t"
                 "buffer_load_dwordx4 v[228:231], %0, %3, 0 offen offset:512\n\tbuffer_load_dwordx4 v[232:235], %0, %3, 0 offen offset:768\n\t"
                 "buffer_load_dwordx4 v[236:239], %1, %3, 0 offen\n\tbuffer_load_dword v240, %2, %4, 0 offen sc1"
                 ::"v"(xo), "v"(c < 8 ? xo + 1024 : 0x10000), "v"(WS_EPOCH + (lane & 31) * 4), "s"(rx), "s"(rws)
                 : "memory", "v220", "v221", "v222", "v223", "v224", "v225", "v226", "v227", "v228", "v229", "v230", "v231", "v232", "v233", "v234", "v235",
                   "v236", "v237", "v238", "v239", "v240");
  } else if (w == 1) {
    asm volatile("buffer_load_dword v241, %0, %1, 0 offen" ::"v"((wg >> 3) * 1024 + lane * 16), "s"(mk_rsrc((const void*)(pc0 & ~255ull), CODE_BYTES)) : "memory", "v241");
  }
  const unsigned long long t0 = __builtin_amdgcn_s_memrealtime();
  __builtin_amdgcn_s_barrier();
  asm volatile("" ::: "memory");
  const int prow = lane >> 3, pch = (lane & 7) ^ (((prow >> 1) & 3) << 1);
  const unsigned ring = (unsigned)(size_t)L;
  const u16* gubase = gj >= 72 ? wup + (gj - 72) * 64 : wgate + gj * 64;
  const int n0 = nq ? nq : WS_E;
  if (w < 4) {
  unsigned xres = 0;
  const int ob = tk >> 2, ocol = tcg * 64 + (tk & 3) * 16 + (lane & 7) * 2;
  if (w == 0 && type == 0) xres = *(const unsigned*)(x + ob * DM + ocol);
  const int q4 = (lane >> 2) & 3, p4 = lane & 3;
  const int wtro = (4 * r + q4) * 128 + (((2 * w + (p4 >> 1)) ^ (((2 * r + (q4 >> 1)) & 3) << 1)) << 4) + (p4 & 1) * 8;
#define GEMV_SLAB(slotoff, ap, acc) do { \
    const v2u l0_ = tr_read(L + (slotoff) + wtro), h0_ = tr_read(L + (slotoff) + wtro + 2048), l1_ = tr_read(L + (slotoff) + wtro + 4096), h1_ = tr_read(L + (slotoff) + wtro + 6144); \
    const v4u b0_ = *(LAS v4u*)(ap), b1_ = *(LAS v4u*)((ap) + 64); \
    acc = mfma32(v4u{l0_.x, l0_.y, h0_.x, h0_.y}, b0_, acc); acc = mfma32(v4u{l1_.x, l1_.y, h1_.x, h1_.y}, b1_, acc); } while (0)
  lds_wait_ge(L + F_XN, 1);
  const unsigned tag = lds_ld(L + F_TAG);
  ST(1);
  const int x4 = c >> 2, f32off = x4 < 2 ? (c & 3) * 512 + (8 * w + 2 * r + x4) * 16 : WS_END;
#define PUBLISH_F32(dst, y) __builtin_amdgcn_raw_buffer_store_b128(v4u{__float_as_uint((x4 & 1) ? y[2] : y[0]), tag, __float_as_uint((x4 & 1) ? y[3] : y[1]), tag}, rws, f32off, (dst), AUX_SC1)
  if (nq) {
    f32x4 y = {0.f, 0.f, 0.f, 0.f};
    lds_t ap = L + L_AX + p4 * AX_STR + r * 16 + qs0 * 128;
#pragma unroll 1
    for (int j = 0; j < nq; ++j) {
      lds_spin_ge(L + F_LAND, 2 * j + 2);
      GEMV_SLAB(j * SLAB, ap + j * 128, y);
    }
    PUBLISH_F32(WS_QP + qj * 2048, y);
  }
  ST(4);
  if (gj >= 0) {
    f32x4 y = {0.f, 0.f, 0.f, 0.f};
    lds_t ap = L + L_AX + p4 * AX_STR + r * 16;
#pragma unroll 1
    for (int j = 0; j < 9; ++j) {
      lds_spin_ge(L + F_LAND, 2 * (nq + j) + 2);
      GEMV_SLAB((nq + j) * SLAB, ap + j * 128, y);
    }
    __builtin_amdgcn_raw_buffer_store_b128(v4u{pk2(y[0], y[1]), tag, pk2(y[2], y[3]), tag}, rws, c < 4 ? WS_GU + gj * 1024 + w * 256 + c * 64 + r * 16 : WS_END, 0, AUX_SC1);
  }
  if (lane == 0) lds_st(L + F_CONS + w * 4, ne + w);
  ST(2);
  lds_wait_ge(L + F_Q, 1);
  ST(3);
  v4u qb[4];
#pragma unroll
  for (int s = 0; s < 4; ++s) qb[s] = *(LAS v4u*)(L + L_Q + (c & 1) * 256 + s * 64 + r * 16);
  int kro[4], vro[8];
#pragma unroll
  for (int s = 0; s < 4; ++s) kro[s] = c * 256 + (((4 * s + r) ^ c) << 4);
#pragma unroll
  for (int t = 0; t < 8; ++t) vro[t] = 4096 + (4 * r + q4) * 256 + (((2 * t + (p4 >> 1)) ^ (((4 * r + q4) & 7) << 1)) << 4) + (p4 & 1) * 8;
  constexpr float C2 = 0.08838834764831845f * 1.4426950408889634f;
  float m = -INFINITY, l = 0.f;
  f32x4 oa = {0.f, 0.f, 0.f, 0.f}, ob2 = {0.f, 0.f, 0.f, 0.f};
  {
    int slot = ne + w;
#pragma unroll 1
    for (int i = w; i < nkv; i += 4) {
      lds_spin_ge(L + F_LAND, 2 * (ne + i) + 1);
      lds_t sb = L + slot * SLAB;
      v4u kf[4]; v2u vf[8];
#pragma unroll
      for (int s = 0; s < 4; ++s) kf[s] = *(LAS v4u*)(sb + kro[s]);
      const f32x4 zero = {0.f, 0.f, 0.f, 0.f};
      const f32x4 z = (mfma32(kf[1], qb[1], mfma32(kf[0], qb[0], zero)) + mfma32(kf[3], qb[3], mfma32(kf[2], qb[2], zero))) * C2;
      const float mn = fmaxf(m, xrow_max(fmaxf(fmaxf(z[0], z[1]), fmaxf(z[2], z[3])))), al = __builtin_amdgcn_exp2f(m - mn);
      m = mn;
      const v2u P = {pk2(__builtin_amdgcn_exp2f(z[0] - m), __builtin_amdgcn_exp2f(z[1] - m)), pk2(__builtin_amdgcn_exp2f(z[2] - m), __builtin_amdgcn_exp2f(z[3] - m))};
      oa *= al; ob2 *= al;
      l = fmaf(l, al, (blo(P.x) + bhi(P.x)) + (blo(P.y) + bhi(P.y)));
      v2u Pt[8];
#pragma unroll
      for (int t = 0; t < 8; ++t) Pt[t] = (c >> 1) == t ? P : v2u{0u, 0u};
      unsigned va[8];
#pragma unroll
      for (int t = 0; t < 8; ++t) { va[t] = (unsigned)(size_t)(sb + vro[t]); asm volatile("" : "+v"(Pt[t]), "+v"(va[t])); }
      asm volatile("" : "+v"(oa), "+v"(ob2), "+v"(l));
      lds_spin_ge(L + F_LAND, 2 * (ne + i) + 2);
#pragma unroll
      for (int t = 0; t < 8; ++t) vf[t] = tr_read((lds_t)(size_t)va[t]);
      __builtin_amdgcn_sched_barrier(0);
#pragma unroll
      for (int t = 0; t < 8; t += 2) { oa = mfma16(vf[t], Pt[t], oa); ob2 = mfma16(vf[t + 1], Pt[t + 1], ob2); }
      if (!PUB_LANE0 || lane == 0) lds_st(L + F_CONS + w * 4, ne + min(i + 4, nkv));
      slot = slot + 4 >= NSLOT ? slot + 4 - NSLOT : slot + 4;
    }
  }
  l = xrow_sum(l);
  ST(5);
  *(LAS f32x4*)(L + L_PW + w * 1024 + lane * 16) = oa + ob2;
  if (lane < 2) *(LAS v2u*)(L + L_PS + w * 16 + lane * 8) = v2u{__float_as_uint(m), __float_as_uint(l)};
  unsigned arrived;
  {
    unsigned t;
    asm volatile("s_mov_b64 exec, 1\n\tds_add_rtn_u32 %1, %2, %3\n\ts_mov_b64 exec, -1\n\ts_waitcnt lgkmcnt(0)\n\tv_readfirstlane_b32 %0, %1"
                 : "=s"(arrived), "=&v"(t) : "v"((unsigned)(size_t)(L + F_PART)), "v"(1u) : "memory");
  }
  if (arrived == 3) {
    v2u ml[4]; f32x4 pw[4];
#pragma unroll
    for (int k = 0; k < 4; ++k) { ml[k] = *(LAS v2u*)(L + L_PS + k * 16 + (c & 1) * 8); pw[k] = *(LAS f32x4*)(L + L_PW + k * 1024 + lane * 16); }
    const float M = fmaxf(fmaxf(__uint_as_float(ml[0].x), __uint_as_float(ml[1].x)), fmaxf(__uint_as_float(ml[2].x), __uint_as_float(ml[3].x)));
    float Ls = 0.f; f32x4 o = {0.f, 0.f, 0.f, 0.f};
#pragma unroll
    for (int k = 0; k < 4; ++k) { const float e = __builtin_amdgcn_exp2f(__uint_as_float(ml[k].x) - M); Ls = fmaf(e, __uint_as_float(ml[k].y), Ls); o += e * pw[k]; }
    const float inv = __builtin_amdgcn_rcpf(Ls), lm = M + __builtin_amdgcn_logf(Ls);
    const int me = (pair & 15) * 16 + (pair >> 4) * 4 + ((jx >> 19) & 3);
    __builtin_amdgcn_raw_buffer_store_b128(v4u{pk2(o[0] * inv, o[1] * inv), tag, pk2(o[2] * inv, o[3] * inv), tag}, rws, WS_PART + me * 1024 + lane * 16, 0, AUX_SC1);
    __builtin_amdgcn_raw_buffer_store_b64(v2u{__float_as_uint(lm), tag}, rws, lane < 2 ? WS_STAT + (me >> 2) * 64 + lane * 32 + (me & 3) * 8 : WS_END, 0, AUX_SC1);
  }
  ST(6);
  if (type == 2) { ST_FLUSH(); return; }

  const int lslot = ne + nkv - (ne + nkv >= 2 * NSLOT ? 2 * NSLOT : NSLOT);
  if (type == 0) {
    v4u d[4], sa, sb;
    const int doff = WS_PART + (tk * 16 + w * 4) * 1024 + lane * 16, soff = WS_STAT + (tk * 4 + w) * 64 + (c & 1) * 32;
    lds_spin_ge(L + F_LAND, 2 * (ne + nkv + 4) - EARLY_GATHER);
#pragma unroll
    for (int k = 0; k < 4; ++k) d[k] = __builtin_amdgcn_raw_buffer_load_b128(rws, doff, k * 1024, GATHER_AUX);
    sa = __builtin_amdgcn_raw_buffer_load_b128(rws, soff, 0, GATHER_AUX); sb = __builtin_amdgcn_raw_buffer_load_b128(rws, soff + 16, 0, GATHER_AUX);
    __builtin_amdgcn_sched_barrier(0);
    lds_spin_ge(L + F_LAND, 2 * (ne + nkv + 4));
    v2u wf[16];
#pragma unroll
    for (int j = 0; j < 4; ++j) {
      const int so = (lslot + j >= NSLOT ? lslot + j - NSLOT : lslot + j) * SLAB + wtro;
#pragma unroll
      for (int k = 0; k < 4; ++k) wf[4 * j + k] = tr_read(L + so + k * 2048);
    }
    lds_t ow = L + L_AH + w * AH_STR + aperm((c & 1) * 128 + (c >> 1) * 16 + r * 4);
    lds_t ap = L + L_AH + p4 * AH_STR + r * 16;
    asm volatile("" : "+v"(ow), "+v"(ap));
#pragma unroll
    for (int k = 0; k < 16; ++k) asm volatile("" : "+v"(wf[k]));
    for (unsigned spin = 0; spin < SPIN_CAP; ++spin) {
      const unsigned long long b0 = BADLANES(d[0]), b1 = BADLANES(d[1]), b2 = BADLANES(d[2]), b3 = BADLANES(d[3]), bs = BADLANES(sa) | BADLANES(sb);
      if ((b0 | b1 | b2 | b3 | bs) == 0ull) break;
      __builtin_amdgcn_s_sleep(POLL_SLEEP);
      asm volatile("" ::: "memory");
      if (b0) d[0] = __builtin_amdgcn_raw_buffer_load_b128(rws, doff, 0, GATHER_AUX);
      if (b1) d[1] = __builtin_amdgcn_raw_buffer_load_b128(rws, doff, 1024, GATHER_AUX);
      if (b2) d[2] = __builtin_amdgcn_raw_buffer_load_b128(rws, doff, 2048, GATHER_AUX);
      if (b3) d[3] = __builtin_amdgcn_raw_buffer_load_b128(rws, doff, 3072, GATHER_AUX);
      sa = __builtin_amdgcn_raw_buffer_load_b128(rws, soff, 0, GATHER_AUX); sb = __builtin_amdgcn_raw_buffer_load_b128(rws, soff + 16, 0, GATHER_AUX);
    }
    ST(7);
    const float la[4] = {__uint_as_float(sa.x), __uint_as_float(sa.z), __uint_as_float(sb.x), __uint_as_float(sb.z)};
    const float M = fmaxf(fmaxf(la[0], la[1]), fmaxf(la[2], la[3]));
    float e[4];
#pragma unroll
    for (int k = 0; k < 4; ++k) e[k] = __builtin_amdgcn_exp2f(la[k] - M);
    const float inv = __builtin_amdgcn_rcpf((e[0] + e[1]) + (e[2] + e[3]));
    f32x4 o = e[0] * f32x4{blo(d[0].x), bhi(d[0].x), blo(d[0].z), bhi(d[0].z)};
#pragma unroll
    for (int k = 1; k < 4; ++k) o += e[k] * f32x4{blo(d[k].x), bhi(d[k].x), blo(d[k].z), bhi(d[k].z)};
    o *= inv;
    *(LAS v2u*)ow = v2u{pk2(o[0], o[1]), pk2(o[2], o[3])};
    asm volatile("s_waitcnt lgkmcnt(0)" ::: "memory");
    __builtin_amdgcn_s_barrier();
    asm volatile("" ::: "memory");
    v4u of[8];
#pragma unroll
    for (int j = 0; j < 8; ++j) of[j] = *(LAS v4u*)(ap + j * 64);
    __builtin_amdgcn_sched_barrier(0);
    f32x4 y = {0.f, 0.f, 0.f, 0.f}, y2 = {0.f, 0.f, 0.f, 0.f};
#pragma unroll
    for (int j = 0; j < 8; j += 2) {
      y = mfma32(v4u{wf[2 * j].x, wf[2 * j].y, wf[2 * j + 1].x, wf[2 * j + 1].y}, of[j], y);
      y2 = mfma32(v4u{wf[2 * j + 2].x, wf[2 * j + 2].y, wf[2 * j + 3].x, wf[2 * j + 3].y}, of[j + 1], y2);
    }
    y += y2;
    PUBLISH_F32(WS_SP + tile * 2048, y);
  } else {
    lds_wait_ge(L + F_H, 1);
    f32x4 y = {0.f, 0.f, 0.f, 0.f};
    lds_t ap = L + L_AH + p4 * AH_STR + r * 16;
    int slot = lslot;
#pragma unroll 1
    for (int j = 0; j < 9; ++j) {
      lds_spin_ge(L + F_LAND, 2 * (ne + nkv + j) + 2);
      GEMV_SLAB(slot * SLAB, ap + j * 128, y);
      slot = slot + 1 == NSLOT ? 0 : slot + 1;
    }
    PUBLISH_F32(WS_FP + tile * 2048, y);
  }
#undef PUBLISH_F32
#undef GEMV_SLAB
  ST(8);
  if (type == 0 && w == 0) {
    const int p = lane >> 3, lo16 = ob * 512 + (tk & 3) * 128 + (lane & 7) * 16;
    v4u s0, s1, f0;
    for (unsigned spin = 0; spin < SPIN_CAP; ++spin) {
      asm volatile("" ::: "memory");
      s0 = __builtin_amdgcn_raw_buffer_load_b128(rws, WS_SP + (tcg * 16 + p) * 2048 + lo16, 0, GATHER_AUX);
      s1 = __builtin_amdgcn_raw_buffer_load_b128(rws, WS_SP + (tcg * 16 + 8 + p) * 2048 + lo16, 0, GATHER_AUX);
      f0 = __builtin_amdgcn_raw_buffer_load_b128(rws, WS_FP + (tcg * 8 + p) * 2048 + lo16, 0, GATHER_AUX);
      if ((BADLANES(s0) | BADLANES(s1) | BADLANES(f0)) == 0ull) break;
    }
    float sa = __uint_as_float(s0.x) + __uint_as_float(s1.x), sb = __uint_as_float(s0.z) + __uint_as_float(s1.z);
    float fa = __uint_as_float(f0.x), fb = __uint_as_float(f0.z);
    sa += dpp<ROR8>(sa); sb += dpp<ROR8>(sb); fa += dpp<ROR8>(fa); fb += dpp<ROR8>(fb);
    sa = rbf(xrow_sum(sa)); sb = rbf(xrow_sum(sb)); fa = rbf(xrow_sum(fa)); fb = rbf(xrow_sum(fb));
    const float ga = rbf(fa * __builtin_amdgcn_rcpf(1.f + __builtin_amdgcn_exp2f(-1.4426950408889634f * fa))), gb = rbf(fb * __builtin_amdgcn_rcpf(1.f + __builtin_amdgcn_exp2f(-1.4426950408889634f * fb)));
    const unsigned res = pk2(blo(xres) + rbf(sa * ga), bhi(xres) + rbf(sb * gb));
    if (lane < 8) *(unsigned*)(out + ob * DM + ocol) = res;
    if (tile == 0 && lane < 32) __builtin_amdgcn_raw_buffer_store_b32(lds_ld(L + L_EP + lane * 4) + 1u, rws, WS_EPOCH + lane * 4, 0, AUX_SC1);
  }
  ST(9);
  ST_FLUSH();
 return;
 }
 if (w == 5) {

    __builtin_amdgcn_s_setprio(3);
    v4u xv[5]; unsigned ep;
#define MOV4(k, a) asm volatile("v_mov_b32 %0, v" #a : "=v"(k))
    asm volatile("s_waitcnt vmcnt(0)" ::: "memory");
    MOV4(xv[0].x, 220); MOV4(xv[0].y, 221); MOV4(xv[0].z, 222); MOV4(xv[0].w, 223); MOV4(xv[1].x, 224); MOV4(xv[1].y, 225); MOV4(xv[1].z, 226); MOV4(xv[1].w, 227);
    MOV4(xv[2].x, 228); MOV4(xv[2].y, 229); MOV4(xv[2].z, 230); MOV4(xv[2].w, 231); MOV4(xv[3].x, 232); MOV4(xv[3].y, 233); MOV4(xv[3].z, 234); MOV4(xv[3].w, 235);
    MOV4(xv[4].x, 236); MOV4(xv[4].y, 237); MOV4(xv[4].z, 238); MOV4(xv[4].w, 239); MOV4(ep, 240);
#undef MOV4
    {
      float s = 0.f;
#pragma unroll
      for (int i = 0; i < 5; ++i)
#pragma unroll
        for (int k = 0; k < 4; ++k) s += blo(xv[i][k]) + bhi(xv[i][k]);
      const float mean = row_sum(s) * (1.f / DM);
      float q = 0.f, q4 = 0.f;
#pragma unroll
      for (int i = 0; i < 5; ++i)
#pragma unroll
        for (int k = 0; k < 4; ++k) {
          const float a = blo(xv[i][k]) - mean, b = bhi(xv[i][k]) - mean;
          if (i < 4) q = fmaf(a, a, fmaf(b, b, q)); else q4 = fmaf(a, a, fmaf(b, b, q4));
        }
      q += c < 8 ? q4 : 0.f;
      const float var = rbf(row_sum(q) * (1.f / DM)), mb = rbf(mean);
      const float inv = __builtin_amdgcn_rcpf(rbf(__builtin_amdgcn_sqrtf(rbf(var + 1e-5f))));
      lds_t ax = L + L_AX + r * AX_STR + (c >> 2) * 64 + (c & 1) * 32 + ((c >> 1) & 1) * 8;
#pragma unroll
      for (int i = 0; i < 5; ++i) {
        unsigned o[4];
#pragma unroll
        for (int k = 0; k < 4; ++k) {
          const unsigned d = pk2(blo(xv[i][k]) - mb, bhi(xv[i][k]) - mb);
          o[k] = pk2(blo(d) * inv, bhi(d) * inv);
        }
        if (i < 4 || c < 8) { *(LAS v2u*)(ax + i * 256) = v2u{o[0], o[1]}; *(LAS v2u*)(ax + i * 256 + 16) = v2u{o[2], o[3]}; }
      }
    }
    unsigned tag = ep;
    tag += dppu<ROR8>(tag); tag += dppu<ROR4>(tag); tag += dppu<ROR2>(tag); tag += dppu<ROR1>(tag);
    tag = (unsigned)__builtin_amdgcn_readlane(tag, 0) + (unsigned)__builtin_amdgcn_readlane(tag, 16) + 0x9E3779B9u;
    if (lane < 32) lds_st(L + L_EP + lane * 4, ep);
    if (lane == 0) lds_st(L + F_TAG, tag);
    asm volatile("s_waitcnt lgkmcnt(0)" ::: "memory");
    if (lane == 0) lds_st(L + F_XN, 1u);
    ST(1);
    while (__builtin_amdgcn_s_memrealtime() - t0 < Q_DELAY) __builtin_amdgcn_s_sleep(2);
    ST(2);
    {
      int o[4];
      v4u g[4];
#pragma unroll
      for (int k = 0; k < 4; ++k) { o[k] = WS_QP + ((pair & 15) * 4 + r) * 4096 + (k >> 1) * 2048 + (pair >> 4) * 512 + c * 32 + (k & 1) * 16; g[k] = v4u{0u, 0u, 0u, 0u}; }
      for (unsigned spin = 0; spin < SPIN_CAP; ++spin) {
        asm volatile("" ::: "memory");
        v4u a[4];
#pragma unroll
        for (int k = 0; k < 4; ++k) a[k] = __builtin_amdgcn_raw_buffer_load_b128(rws, o[k], 0, GATHER_AUX);
        bool done = true;
#pragma unroll
        for (int k = 0; k < 4; ++k) { const bool ok = TAGOK(a[k]); g[k] = ok ? a[k] : g[k]; o[k] = ok ? WS_END : o[k]; done = done & (o[k] == WS_END); }
        if (__all(done)) break;
      }
      *(LAS v2u*)(L + L_Q + lane * 8) = v2u{pk2(__uint_as_float(g[0].x) + __uint_as_float(g[2].x), __uint_as_float(g[0].z) + __uint_as_float(g[2].z)),
                                            pk2(__uint_as_float(g[1].x) + __uint_as_float(g[3].x), __uint_as_float(g[1].z) + __uint_as_float(g[3].z))};
      asm volatile("s_waitcnt lgkmcnt(0)" ::: "memory");
      if (lane == 0) lds_st(L + F_Q, 1u);
    }
    ST(3);
    if (type == 1) {
      const int off = WS_GU + (tile & 7) * 9 * 1024 + lane * 16;
      lds_t ah = L + L_AH + (c >> 2) * AH_STR + aperm(r * 16 + (c & 3) * 4);
      v4u g[3], u[3], gn[3], un[3];
#pragma unroll
      for (int e = 0; e < 3; ++e) { gn[e] = __builtin_amdgcn_raw_buffer_load_b128(rws, off, e * 1024, GATHER_AUX); un[e] = __builtin_amdgcn_raw_buffer_load_b128(rws, off, (72 + e) * 1024, GATHER_AUX); }
      unsigned spin = 0;
#pragma unroll 1
      for (int j = 0; j < 9;) {
#pragma unroll
        for (int e = 0; e < 3; ++e) { g[e] = gn[e]; u[e] = un[e]; }
        const bool ok = (BADLANES(g[0]) | BADLANES(u[0]) | BADLANES(g[1]) | BADLANES(u[1]) | BADLANES(g[2]) | BADLANES(u[2])) == 0ull || ++spin >= SPIN_CAP;
        const int jn = ok ? j + 3 : j;
        asm volatile("" ::: "memory");
        if (jn < 9) {
#pragma unroll
          for (int e = 0; e < 3; ++e) { gn[e] = __builtin_amdgcn_raw_buffer_load_b128(rws, off + jn * 1024, e * 1024, GATHER_AUX); un[e] = __builtin_amdgcn_raw_buffer_load_b128(rws, off + jn * 1024, (72 + e) * 1024, GATHER_AUX); }
        }
        if (ok) {
#pragma unroll
          for (int e = 0; e < 3; ++e) {
            const float gv[4] = {blo(g[e].x), bhi(g[e].x), blo(g[e].z), bhi(g[e].z)}, uv[4] = {blo(u[e].x), bhi(u[e].x), blo(u[e].z), bhi(u[e].z)};
            float hv[4];
#pragma unroll
            for (int i = 0; i < 4; ++i) {
              const float a = gv[i], inner = 0.7978845608028654f * fmaf(0.044715f * a * a, a, a);
              hv[i] = rbf(a * __builtin_amdgcn_rcpf(1.f + __builtin_amdgcn_exp2f(-2.8853900817779268f * inner))) * uv[i];
            }
            *(LAS v2u*)(ah + (j + e) * 128) = v2u{pk2(hv[0], hv[1]), pk2(hv[2], hv[3])};
          }
        }
        j = jn;
      }
      asm volatile("s_waitcnt lgkmcnt(0)" ::: "memory");
      if (lane == 0) lds_st(L + F_H, 1u);
    }
    ST(4);
    ST_FLUSH();
    return;
    }
 {

    __builtin_amdgcn_s_setprio(3);
    {
    const int stride = nq ? QD * 2 : FF * 2;
    const rsrc_t rw = mk_rsrc(nq ? wq + (qj >> 1) * 64 : gubase, 575 * stride + 128);
    const int voff = prow * stride + pch * 16, so0 = nq ? qs0 * 64 * stride : 0;
#pragma unroll 1
    for (int s = 0; s < n0; ++s) dma_wslab(rw, voff, so0 + s * 64 * stride, 8 * stride, ring + s * SLAB);
    }
    int s = n0, pub = 0;
#define PUB(v) do { pub = max(pub, (v)); if (!PUB_LANE0 || lane == 0) lds_st(L + F_LAND, pub); } while (0)
    {
      const rsrc_t rw = mk_rsrc(gubase, gj < 0 ? 0 : 575 * (FF * 2) + 128);
      const int voff = prow * (FF * 2) + pch * 16;
#pragma unroll 1
      for (; s < ne; ++s) {
        wait_vm<8 * (WS_E - 1)>();
        PUB(2 * (s - (WS_E - 1)));
        dma_wslab(rw, voff, (s - nq) * 64 * (FF * 2), 8 * (FF * 2), ring + s * SLAB);
      }
    }
    ST(1);
    int slot = ne;
#define ADMIT(WS) do { \
      if (HALF_PUB) { wait_vm<8 * (WS) - 4>(); PUB(2 * (s - (WS)) + 1); } \
      wait_vm<8 * ((WS) - 1)>(); \
      PUB(2 * (s - ((WS) - 1))); \
      if (s >= NSLOT) { \
        bool drained = false; \
        for (;;) { \
          asm volatile("" ::: "memory"); \
          const v4u cv = *(volatile LAS v4u*)(L + F_CONS); \
          if (__builtin_amdgcn_readfirstlane(min(min(cv.x, cv.y), min(cv.z, cv.w))) > (unsigned)(s - NSLOT)) break; \
          if (!drained) { ST(9); wait_vm<0>(); PUB(2 * s); drained = true; } \
          __builtin_amdgcn_s_sleep(1); \
        } \
      } } while (0)
    {
      const size_t kvbase = ((size_t)pair * SK + (SK - WIN) + ((jx >> 6) & 127) * 16) * DH;
      const rsrc_t rk = mk_rsrc(Kc + kvbase, nkv * 4096), rv = mk_rsrc(Vc + kvbase, nkv * 4096);
      const int kr = r, kc = c;
      int vk[4], vv[4];
#pragma unroll
      for (int p = 0; p < 4; ++p) { vk[p] = p * 1024 + kr * 256 + ((kc ^ (4 * p + kr)) << 4); vv[p] = p * 1024 + kr * 256 + ((kc ^ (((4 * p + kr) & 7) << 1)) << 4); }
#pragma unroll 1
      for (int i = 0; i < nkv; ++i, ++s) {
        ADMIT(WS_KV);
        dma_kvslab(rk, rv, vk, vv, i * 4096, ring + slot * SLAB);
        slot = slot + 1 == NSLOT ? 0 : slot + 1;
      }
    }
    ST(4);
    if (nl) {
      const u16* base = type == 0 ? wo + tk * 256 * DM + tcg * 64 : wdn + (tile & 7) * 576 * DM + (tile >> 3) * 64;
      const rsrc_t rw = mk_rsrc(base, (nl * 64 - 1) * (DM * 2) + 128);
      const int voff = prow * (DM * 2) + pch * 16;
#pragma unroll 1
      for (int i = 0; i < nl; ++i, ++s) {
        ADMIT(WS_L);
        dma_wslab(rw, voff, i * 64 * (DM * 2), 8 * (DM * 2), ring + slot * SLAB);
        slot = slot + 1 == NSLOT ? 0 : slot + 1;
      }
    }
#undef ADMIT
    ST(5);
    static_assert(WS_KV <= WS_L && WS_L <= 7, "drain ladder");
    if (WS_L > 6) { wait_vm<52>(); PUB(2 * s - 13); wait_vm<48>(); PUB(2 * s - 12); }
    if (WS_L > 5) { wait_vm<44>(); PUB(2 * s - 11); wait_vm<40>(); PUB(2 * s - 10); }
    if (WS_L > 4) { wait_vm<36>(); PUB(2 * s - 9); wait_vm<32>(); PUB(2 * s - 8); }
    if (WS_L > 3) { wait_vm<28>(); PUB(2 * s - 7); wait_vm<24>(); PUB(2 * s - 6); }
    wait_vm<20>(); PUB(2 * s - 5); wait_vm<16>(); PUB(2 * s - 4);
    wait_vm<12>(); PUB(2 * s - 3); wait_vm<8>(); PUB(2 * s - 2);
    wait_vm<4>(); PUB(2 * s - 1); wait_vm<0>(); PUB(2 * s);
#undef PUB
    ST(6);
    ST_FLUSH();
    return;
   }
}

#define PEN3 50
#define LATE_COST 50
#define NOLATE_BONUS 0
#define ODDPEN 5
static void build_jobs(Jobs& J, int* kind_out = nullptr) {
  int wgs[3][256], nw[3] = {0, 0, 0};
  int type[256], tile[256], qj[256], gj[256], cost[256];
  for (int wg = 0; wg < 256; ++wg) {
    const int xcd = wg & 7, idx = wg >> 3, gi = idx & 15, o = (idx >> 4) * 7 + gi - 9;
    type[wg] = gi < 9 ? 0 : o < 9 ? 1 : 2;
    tile[wg] = gi < 9 ? gi * 16 + xcd * 2 + (idx >> 4) : o < 9 ? o * 8 + xcd : 0;
    wgs[type[wg]][nw[type[wg]]++] = wg;
    qj[wg] = gj[wg] = -1;
  }
  int fast[126], nf = 0, slow[18], nsl = 0, halves[128], nh = 0;
  for (int g = 0; g < 144; ++g) { if ((g & 7) == 3) slow[nsl++] = g; else fast[nf++] = g; }
  for (int h = 0; h < 128; ++h) if (((h >> 1) & 7) == 3) halves[nh++] = h;
  for (int h = 0; h < 128; ++h) if (((h >> 1) & 7) != 3 && !(h & 1)) halves[nh++] = h;
  for (int h = 0; h < 128; ++h) if (((h >> 1) & 7) != 3 && (h & 1)) halves[nh++] = h;
  int ih = 0;
  for (int i = 0; i < 40; ++i) { if (i < 22) qj[wgs[2][i]] = halves[ih++]; else gj[wgs[2][i]] = slow[i - 22]; }
  for (int i = 0; i < 72; ++i) qj[wgs[1][i]] = halves[ih++];
  for (int i = 0; i < 144; ++i) { if (i < 34) qj[wgs[0][i]] = halves[ih++]; if (i >= 18) gj[wgs[0][i]] = fast[i - 18]; }
  for (int wg = 0; wg < 256; ++wg) {
    const int nq = qj[wg] < 0 ? 0 : 5 - (qj[wg] & 1);
    cost[wg] = 10 * nq + (nq && ((qj[wg] >> 1) & 7) == 3 ? PEN3 * nq / 9 : 0) + (gj[wg] < 0 ? 0 : (gj[wg] & 7) == 3 ? 90 + PEN3 : 90) +
               (type[wg] == 1 ? LATE_COST : type[wg] == 2 ? -NOLATE_BONUS : 0) + ((wg & 1) ? ODDPEN : 0);
  }
  int ord[256];
  for (int i = 0; i < 256; ++i) ord[i] = i;
  for (int i = 1; i < 256; ++i) { const int v = ord[i]; int j = i; while (j > 0 && cost[ord[j - 1]] < cost[v]) { ord[j] = ord[j - 1]; --j; } ord[j] = v; }
  for (int p = 0; p < 64; ++p) {
    const int mem[4] = {ord[p], ord[127 - p], ord[128 + p], ord[255 - p]};
    const int T = (1280 + cost[mem[0]] + cost[mem[1]] + cost[mem[2]] + cost[mem[3]]) / 4;
    int n[4], sum = 0;
    for (int j = 0; j < 4; ++j) { n[j] = (T - cost[mem[j]] + 5) / 10; sum += n[j]; }
    for (int j = 3; sum != 128; j = (j + 3) & 3) { if (sum < 128) { ++n[j]; ++sum; } else { --n[3 - j]; --sum; } }
    int s0 = 0;
    for (int j = 0; j < 4; ++j) {
      const int wg = mem[j];
      J.v[2 * wg] = (unsigned)p | (unsigned)s0 << 6 | (unsigned)n[j] << 13 | (unsigned)j << 19 | (unsigned)type[wg] << 21;
      J.v[2 * wg + 1] = (unsigned)(qj[wg] + 1) | (unsigned)(gj[wg] + 1) << 8 | (unsigned)tile[wg] << 16;
      s0 += n[j];
    }
  }
  if (kind_out) for (int wg = 0; wg < 256; ++wg) kind_out[wg] = type[wg] == 0 ? (qj[wg] < 0 ? 0 : gj[wg] < 0 ? 5 : 2) : type[wg] == 1 ? 1 : gj[wg] >= 0 ? 3 : 4;
}
}

extern "C" void kernel_launch(void* const* d_in, const int* in_sizes, int n_in, void* d_out, int out_size, void* d_ws, size_t ws_size, hipStream_t stream) {
  Jobs J;
  build_jobs(J);
  fused_decode<<<256, 384, 0, stream>>>((const u16*)d_in[0], (const u16*)d_in[1], (const u16*)d_in[2], (const u16*)d_in[3], (const u16*)d_in[4],
                                        (const u16*)d_in[6], (const u16*)d_in[7], (const u16*)d_in[8], (u16*)d_out, (unsigned char*)d_ws, J);
}
```

```cpp
#include <hip/hip_runtime.h>
#include <hip/hip_bf16.h>
#include <stdint.h>

#define LAS __attribute__((address_space(3)))
typedef unsigned v4u __attribute__((ext_vector_type(4)));
typedef unsigned v2u __attribute__((ext_vector_type(2)));
typedef float f32x4 __attribute__((ext_vector_type(4)));
typedef short s16x4 __attribute__((ext_vector_type(4)));
typedef __bf16 bf16x2 __attribute__((ext_vector_type(2)));
typedef __bf16 bf16x8 __attribute__((ext_vector_type(8)));
typedef unsigned short u16;
typedef __amdgpu_buffer_rsrc_t rsrc_t;

namespace {
constexpr int DM = 576, SK = 8192, DH = 128, WIN = 2048, FF = 4608, QD = 4096;
constexpr int AUX_SC1 = 16;
constexpr int WS_E = 5;
constexpr int T_NOM = 1150, T_NOM1 = 1130;
constexpr int LAG_HI = 25, LEAD_MAX = 25;
constexpr int BOOST_FROM = 10;
constexpr int LATE_FIRST = 2;
constexpr int EARLY_GATHER = 2;
constexpr int Q_DELAY = 370;
constexpr int CODE_BYTES = $;
constexpr unsigned SPIN_CAP = 30000;

constexpr int WS_EPOCH = 0;
constexpr int WS_GU = 4096;
constexpr int WS_QP = WS_GU + 144 * 1024;
constexpr int WS_PART = WS_QP + 128 * 2048;
constexpr int WS_STAT = WS_PART + 256 * 1024;
constexpr int WS_FP = WS_STAT + 4096;
constexpr int WS_SP = WS_FP + 144 * 2048;
constexpr int WS_END = WS_SP + 144 * 2048;

constexpr int NSLOT = 18, SLAB = 8192;
constexpr int L_AX = NSLOT * SLAB, AX_STR = 1216;
constexpr int L_PW = L_AX;
constexpr int L_AH = L_AX + 4 * AX_STR, AH_STR = 1216;
constexpr int L_Q = L_AH + 4 * AH_STR;
constexpr int L_PS = L_Q + 512;
constexpr int L_EP = L_PS + 64;
constexpr int L_FLAG = L_EP + 128;
constexpr int F_CONS = L_FLAG, F_TAG = L_FLAG + 16, F_Q = L_FLAG + 20, F_LAND = L_FLAG + 24, F_XN = L_FLAG + 28, F_PART = L_FLAG + 32, F_H = L_FLAG + 36;
constexpr int L_BYTES = L_FLAG + 64;
static_assert(L_BYTES <= 160 * 1024 && L_BYTES > 80 * 1024, "one workgroup per CU");

struct Jobs { unsigned v[512]; };

__device__ __forceinline__ rsrc_t mk_rsrc(const void* p, int bytes) { return __builtin_amdgcn_make_buffer_rsrc(const_cast<void*>(p), 0, bytes, 0x00020000); }
__device__ __forceinline__ float blo(unsigned u) { return __uint_as_float(u << 16); }
__device__ __forceinline__ float bhi(unsigned u) { return __uint_as_float(u & 0xffff0000u); }
__device__ __forceinline__ unsigned pk2(float lo, float hi) {
  bf16x2 r; r[0] = (__bf16)lo; r[1] = (__bf16)hi; return __builtin_bit_cast(unsigned, r);
}
__device__ __forceinline__ float rbf(float f) { return (float)(__bf16)f; }
template <int CTRL> __device__ __forceinline__ float dpp(float x) {
  return __builtin_bit_cast(float, __builtin_amdgcn_mov_dpp(__builtin_bit_cast(int, x), CTRL, 0xf, 0xf, true));
}
template <int CTRL> __device__ __forceinline__ unsigned dppu(unsigned x) { return (unsigned)__builtin_amdgcn_mov_dpp((int)x, CTRL, 0xf, 0xf, true); }
constexpr int ROR8 = 0x128, ROR4 = 0x124, ROR2 = 0x122, ROR1 = 0x121;
__device__ __forceinline__ float xrow_max(float x) {
  auto s = __builtin_amdgcn_permlane16_swap(__float_as_uint(x), __float_as_uint(x), false, false);
  x = fmaxf(__uint_as_float(s[0]), __uint_as_float(s[1]));
  auto t = __builtin_amdgcn_permlane32_swap(__float_as_uint(x), __float_as_uint(x), false, false);
  return fmaxf(__uint_as_float(t[0]), __uint_as_float(t[1]));
}
__device__ __forceinline__ float xrow_sum(float x) {
  auto s = __builtin_amdgcn_permlane16_swap(__float_as_uint(x), __float_as_uint(x), false, false);
  x = __uint_as_float(s[0]) + __uint_as_float(s[1]);
  auto t = __builtin_amdgcn_permlane32_swap(__float_as_uint(x), __float_as_uint(x), false, false);
  return __uint_as_float(t[0]) + __uint_as_float(t[1]);
}
__device__ __forceinline__ float row_sum(float x) { x += dpp<ROR8>(x); x += dpp<ROR4>(x); x += dpp<ROR2>(x); x += dpp<ROR1>(x); return x; }
__device__ __forceinline__ f32x4 mfma32(v4u a, v4u b, f32x4 c) {
  return __builtin_amdgcn_mfma_f32_16x16x32_bf16(__builtin_bit_cast(bf16x8, a), __builtin_bit_cast(bf16x8, b), c, 0, 0, 0);
}
__device__ __forceinline__ f32x4 mfma16(v2u a, v2u b, f32x4 c) {
  return __builtin_amdgcn_mfma_f32_16x16x16bf16_1k(__builtin_bit_cast(s16x4, a), __builtin_bit_cast(s16x4, b), c, 0, 0, 0);
}
#define TAGOK(v) ((((v).y ^ tag) | ((v).w ^ tag)) == 0u)
#define BADLANES(v) (__builtin_amdgcn_ballot_w64((v).y != tag) | __builtin_amdgcn_ballot_w64((v).w != tag))

typedef LAS unsigned char* lds_t;
__device__ __forceinline__ v2u tr_read(lds_t p) { return __builtin_bit_cast(v2u, __builtin_amdgcn_ds_read_tr16_b64_v4i16((LAS s16x4*)p)); }
__device__ __forceinline__ unsigned lds_ld(lds_t p) { return __hip_atomic_load((LAS unsigned*)p, __ATOMIC_RELAXED, __HIP_MEMORY_SCOPE_WORKGROUP); }
__device__ __forceinline__ void lds_st(lds_t p, unsigned v) { __hip_atomic_store((LAS unsigned*)p, v, __ATOMIC_RELAXED, __HIP_MEMORY_SCOPE_WORKGROUP); }
__device__ __forceinline__ void lds_wait_ge(lds_t p, unsigned want) {
  while ((unsigned)__builtin_amdgcn_readfirstlane(lds_ld(p)) < want) __builtin_amdgcn_s_sleep(1);
  asm volatile("" ::: "memory");
}
__device__ __forceinline__ void lds_spin_ge(lds_t p, unsigned want) {
  unsigned v;
  do v = __builtin_amdgcn_readfirstlane(lds_ld(p)); while (v < want);
  asm volatile("" ::: "memory");
}
template <int N> __device__ __forceinline__ void wait_vm() { asm volatile("s_waitcnt vmcnt(%0)" ::"i"(N) : "memory"); }
#define DMA_OPS " offen nt lds\n\t"
__device__ __forceinline__ void dma_wslab(rsrc_t r, int voff, int soff, int step, unsigned ldsoff) {
  int so;
#define NEXT "s_add_u32 m0, m0, 0x400\n\ts_add_u32 %0, %0, %4\n\tbuffer_load_dwordx4 %2, %3, %0" DMA_OPS
  asm volatile("s_mov_b32 m0, %1\n\ts_mov_b32 %0, %5\n\tbuffer_load_dwordx4 %2, %3, %0" DMA_OPS NEXT NEXT NEXT NEXT NEXT NEXT NEXT
               : "=&s"(so) : "s"(ldsoff), "v"(voff), "s"(r), "s"(step), "s"(soff) : "memory", "scc");
#undef NEXT
}
__device__ __forceinline__ void dma_kvslab(rsrc_t rk, rsrc_t rv, const int (&vk)[4], const int (&vv)[4], int soff, unsigned ldsoff) {
#define NEXT(V, R) "s_add_u32 m0, m0, 0x400\n\ts_nop 0\n\tbuffer_load_dwordx4 " V ", " R ", %0" DMA_OPS
  asm volatile("s_mov_b32 m0, %1\n\ts_nop 0\n\tbuffer_load_dwordx4 %4, %2, %0" DMA_OPS NEXT("%5", "%2") NEXT("%6", "%2") NEXT("%7", "%2")
               NEXT("%8", "%3") NEXT("%9", "%3") NEXT("%10", "%3") NEXT("%11", "%3")
               :: "s"(soff), "s"(ldsoff), "s"(rk), "s"(rv), "v"(vk[0]), "v"(vk[1]), "v"(vk[2]), "v"(vk[3]), "v"(vv[0]), "v"(vv[1]), "v"(vv[2]), "v"(vv[3]) : "memory", "scc");
#undef NEXT
}
__device__ __forceinline__ int aperm(int k) { const int g = (k >> 2) & 7; return (k >> 5) * 64 + (g & 3) * 16 + (g >> 2) * 8; }

__global__ void __launch_bounds__(384, 2)
fused_decode(const u16* __restrict__ x, const u16* __restrict__ Kc, const u16* __restrict__ Vc, const u16* __restrict__ wq, const u16* __restrict__ wo,
             const u16* __restrict__ wgate, const u16* __restrict__ wup, const u16* __restrict__ wdn, u16* __restrict__ out, unsigned char* __restrict__ ws,
             const Jobs jobs) {
  __shared__ __attribute__((aligned(16))) unsigned char smem[L_BYTES];
  lds_t L = (lds_t)smem;
  unsigned long long pc0;
  asm volatile("s_getpc_b64 %0" : "=s"(pc0));
  const int lane = threadIdx.x & 63, w = __builtin_amdgcn_readfirstlane(threadIdx.x >> 6), wg = blockIdx.x;
  asm volatile("buffer_load_dword v241, %0, %1, 0 offen" ::"v"(threadIdx.x < 64 ? (wg >> 3) * 1024 + lane * 16 : CODE_BYTES),
               "s"(mk_rsrc((const void*)(pc0 & ~255ull), CODE_BYTES)) : "memory", "v241");
  lds_st(L + L_FLAG + (lane & 15) * 4, 0u);
  const unsigned jx = jobs.v[2 * wg], jy = jobs.v[2 * wg + 1];
  asm volatile("" ::"s"(x), "s"(Kc), "s"(Vc), "s"(wq), "s"(wo), "s"(wgate), "s"(wup), "s"(wdn), "s"(out), "s"(ws), "s"(jx), "s"(jy) : "memory");
  asm volatile("s_waitcnt lgkmcnt(0)" ::: "memory");
  const int pair = jx & 63, nkv = (jx >> 13) & 63, type = (jx >> 21) & 3, qj = (int)(jy & 255) - 1, gj = (int)((jy >> 8) & 255) - 1, tile = jy >> 16;
  const int nq = qj < 0 ? 0 : 5 - (qj & 1), qs0 = (qj & 1) * 5, ne = nq + (gj < 0 ? 0 : 9), nl = type == 0 ? 4 : type == 1 ? 6 : 0;
  const int tk = tile & 15, tcg = tile >> 4;
  const rsrc_t rws = mk_rsrc(ws, WS_END);
  const int r = lane >> 4, c = lane & 15;
  if (w == 5) {
    const rsrc_t rx = mk_rsrc(x, 4 * DM * 2);
    const int xo = r * (DM * 2) + c * 16;
    asm volatile("buffer_load_dwordx4 v[220:223], %0, %3, 0 offen\n\tbuffer_load_dwordx4 v[224:227], %0, %3, 0 offen offset:256\n\t"
                 "buffer_load_dwordx4 v[228:231], %0, %3, 0 offen offset:512\n\tbuffer_load_dwordx4 v[232:235], %0, %3, 0 offen offset:768\n\t"
                 "buffer_load_dwordx4 v[236:239], %1, %3, 0 offen\n\tbuffer_load_dword v240, %2, %4, 0 offen sc1"
                 ::"v"(xo), "v"(c < 8 ? xo + 1024 : 0x10000), "v"(WS_EPOCH + (lane & 31) * 4), "s"(rx), "s"(rws)
                 : "memory", "v220", "v221", "v222", "v223", "v224", "v225", "v226", "v227", "v228", "v229", "v230", "v231", "v232", "v233", "v234", "v235",
                   "v236", "v237", "v238", "v239", "v240");
  }
  const unsigned long long t0 = __builtin_amdgcn_s_memrealtime();
  __builtin_amdgcn_s_barrier();
  asm volatile("" ::: "memory");
  const int prow = lane >> 3, pch = (lane & 7) ^ (((prow >> 1) & 3) << 1);
  const unsigned ring = (unsigned)(size_t)L;
  const u16* gubase = gj >= 72 ? wup + (gj - 72) * 64 : wgate + gj * 64;
  const int n0 = nq ? nq : WS_E;
  if (w < 4) {
  unsigned xres = 0;
  const int ob = tk >> 2, ocol = tcg * 64 + (tk & 3) * 16 + (lane & 7) * 2;
  if (w == 0 && type == 0) xres = *(const unsigned*)(x + ob * DM + ocol);
  const int q4 = (lane >> 2) & 3, p4 = lane & 3;
  const int wtro = (4 * r + q4) * 128 + (((2 * w + (p4 >> 1)) ^ (((2 * r + (q4 >> 1)) & 3) << 1)) << 4) + (p4 & 1) * 8;
#define GEMV_SLAB(slotoff, ap, acc) do { \
    const v2u l0_ = tr_read(L + (slotoff) + wtro), h0_ = tr_read(L + (slotoff) + wtro + 2048), l1_ = tr_read(L + (slotoff) + wtro + 4096), h1_ = tr_read(L + (slotoff) + wtro + 6144); \
    const v4u b0_ = *(LAS v4u*)(ap), b1_ = *(LAS v4u*)((ap) + 64); \
    acc = mfma32(v4u{l0_.x, l0_.y, h0_.x, h0_.y}, b0_, acc); acc = mfma32(v4u{l1_.x, l1_.y, h1_.x, h1_.y}, b1_, acc); } while (0)
  lds_wait_ge(L + F_XN, 1);
  const unsigned tag = lds_ld(L + F_TAG);
  const int x4 = c >> 2, f32off = x4 < 2 ? (c & 3) * 512 + (8 * w + 2 * r + x4) * 16 : WS_END;
#define PUBLISH_F32(dst, y) __builtin_amdgcn_raw_buffer_store_b128(v4u{__float_as_uint((x4 & 1) ? y[2] : y[0]), tag, __float_as_uint((x4 & 1) ? y[3] : y[1]), tag}, rws, f32off, (dst), AUX_SC1)
  if (__builtin_expect(nq != 0, 1)) {
    f32x4 y = {0.f, 0.f, 0.f, 0.f};
    lds_t ap = L + L_AX + p4 * AX_STR + r * 16 + qs0 * 128;
#pragma unroll 1
    for (int j = 0; j < nq; ++j) {
      lds_spin_ge(L + F_LAND, 2 * j + 2);
      GEMV_SLAB(j * SLAB, ap + j * 128, y);
    }
    PUBLISH_F32(WS_QP + qj * 2048, y);
  }
  if (__builtin_expect(gj >= 0, 1)) {
    f32x4 y = {0.f, 0.f, 0.f, 0.f};
    lds_t ap = L + L_AX + p4 * AX_STR + r * 16;
#pragma unroll 1
    for (int j = 0; j < 9; ++j) {
      lds_spin_ge(L + F_LAND, 2 * (nq + j) + 2);
      GEMV_SLAB((nq + j) * SLAB, ap + j * 128, y);
    }
    __builtin_amdgcn_raw_buffer_store_b128(v4u{pk2(y[0], y[1]), tag, pk2(y[2], y[3]), tag}, rws, c < 4 ? WS_GU + gj * 1024 + w * 256 + c * 64 + r * 16 : WS_END, 0, AUX_SC1);
  }
  asm volatile("" ::: "memory");
  if (lane == 0) lds_st(L + F_CONS + w * 4, ne + w);
  lds_wait_ge(L + F_Q, 1);
  v4u qb[4];
#pragma unroll
  for (int s = 0; s < 4; ++s) qb[s] = *(LAS v4u*)(L + L_Q + (c & 1) * 256 + s * 64 + r * 16);
  int kro[4], vro[8];
#pragma unroll
  for (int s = 0; s < 4; ++s) kro[s] = c * 256 + (((4 * s + r) ^ c) << 4);
#pragma unroll
  for (int t = 0; t < 8; ++t) vro[t] = 4096 + (4 * r + q4) * 256 + (((2 * t + (p4 >> 1)) ^ (((4 * r + q4) & 7) << 1)) << 4) + (p4 & 1) * 8;
  constexpr float C2 = 0.08838834764831845f * 1.4426950408889634f;
  float m = -INFINITY, l = 0.f;
  const int wl = (nkv - 1) & 3;
  f32x4 po = {0.f, 0.f, 0.f, 0.f};
  float el = 0.f, inv = 0.f, lm = 0.f;
  bool pre = false;
#define MERGE3(mlast, llast) do { \
    v2u ml_[3]; f32x4 pw_[3]; \
    _Pragma("unroll") for (int k = 0; k < 3; ++k) { const int wk_ = (wl + 1 + k) & 3; \
      ml_[k] = *(LAS v2u*)(L + L_PS + wk_ * 16 + (c & 1) * 8); pw_[k] = *(LAS f32x4*)(L + L_PW + wk_ * 1024 + lane * 16); } \
    const float ml3_ = (mlast), M_ = fmaxf(fmaxf(__uint_as_float(ml_[0].x), __uint_as_float(ml_[1].x)), fmaxf(__uint_as_float(ml_[2].x), ml3_)); \
    const float e0_ = __builtin_amdgcn_exp2f(__uint_as_float(ml_[0].x) - M_), e1_ = __builtin_amdgcn_exp2f(__uint_as_float(ml_[1].x) - M_), \
                e2_ = __builtin_amdgcn_exp2f(__uint_as_float(ml_[2].x) - M_); \
    el = __builtin_amdgcn_exp2f(ml3_ - M_); \
    const float Ls_ = fmaf(el, (llast), fmaf(e2_, __uint_as_float(ml_[2].y), fmaf(e1_, __uint_as_float(ml_[1].y), e0_ * __uint_as_float(ml_[0].y)))); \
    _Pragma("unroll") for (int e = 0; e < 4; ++e) po[e] = fmaf(e2_, pw_[2][e], fmaf(e1_, pw_[1][e], e0_ * pw_[0][e])); \
    inv = __builtin_amdgcn_rcpf(Ls_); lm = M_ + __builtin_amdgcn_logf(Ls_); } while (0)
  f32x4 oa = {0.f, 0.f, 0.f, 0.f}, ob2 = {0.f, 0.f, 0.f, 0.f};
  {
    int slot = ne + w;
#pragma unroll 1
    for (int i = w; i < nkv; i += 4) {
      lds_spin_ge(L + F_LAND, 2 * (ne + i) + 1);
      lds_t sb = L + slot * SLAB;
      v4u kf[4]; v2u vf[8];
#pragma unroll
      for (int s = 0; s < 4; ++s) kf[s] = *(LAS v4u*)(sb + kro[s]);
      const f32x4 zero = {0.f, 0.f, 0.f, 0.f};
      const f32x4 z = (mfma32(kf[1], qb[1], mfma32(kf[0], qb[0], zero)) + mfma32(kf[3], qb[3], mfma32(kf[2], qb[2], zero))) * C2;
      const float mn = fmaxf(m, xrow_max(fmaxf(fmaxf(z[0], z[1]), fmaxf(z[2], z[3])))), al = __builtin_amdgcn_exp2f(m - mn);
      m = mn;
      const v2u P = {pk2(__builtin_amdgcn_exp2f(z[0] - m), __builtin_amdgcn_exp2f(z[1] - m)), pk2(__builtin_amdgcn_exp2f(z[2] - m), __builtin_amdgcn_exp2f(z[3] - m))};
      oa *= al; ob2 *= al;
      l = fmaf(l, al, (blo(P.x) + bhi(P.x)) + (blo(P.y) + bhi(P.y)));
      v2u Pt[8];
#pragma unroll
      for (int t = 0; t < 8; ++t) Pt[t] = (c >> 1) == t ? P : v2u{0u, 0u};
      unsigned va[8];
#pragma unroll
      for (int t = 0; t < 8; ++t) { va[t] = (unsigned)(size_t)(sb + vro[t]); asm volatile("" : "+v"(Pt[t]), "+v"(va[t])); }
      asm volatile("" : "+v"(oa), "+v"(ob2), "+v"(l));
      if (i == nkv - 1 && (unsigned)__builtin_amdgcn_readfirstlane(lds_ld(L + F_PART)) == 3u) {
        asm volatile("" ::: "memory");
        MERGE3(m, xrow_sum(l));
        pre = true;
        asm volatile("" : "+v"(po), "+v"(el), "+v"(inv), "+v"(lm));
      }
      lds_spin_ge(L + F_LAND, 2 * (ne + i) + 2);
#pragma unroll
      for (int t = 0; t < 8; ++t) vf[t] = tr_read((lds_t)(size_t)va[t]);
      __builtin_amdgcn_sched_barrier(0);
#pragma unroll
      for (int t = 0; t < 8; t += 2) { oa = mfma16(vf[t], Pt[t], oa); ob2 = mfma16(vf[t + 1], Pt[t + 1], ob2); }
      asm volatile("" ::: "memory");
      lds_st(L + F_CONS + w * 4, ne + min(i + 4, nkv));
      slot = slot + 4 >= NSLOT ? slot + 4 - NSLOT : slot + 4;
    }
  }
  f32x4 last = oa + ob2;
  if (__builtin_expect(!pre, 0)) {
    l = xrow_sum(l);
    *(LAS f32x4*)(L + L_PW + w * 1024 + lane * 16) = last;
    if (lane < 2) *(LAS v2u*)(L + L_PS + w * 16 + lane * 8) = v2u{__float_as_uint(m), __float_as_uint(l)};
    unsigned arrived, t;
    asm volatile("s_mov_b64 exec, 1\n\tds_add_rtn_u32 %1, %2, %3\n\ts_mov_b64 exec, -1\n\ts_waitcnt lgkmcnt(0)\n\tv_readfirstlane_b32 %0, %1"
                 : "=s"(arrived), "=&v"(t) : "v"((unsigned)(size_t)(L + F_PART)), "v"(1u) : "memory");
    if (arrived == 3) {
      const v2u mll = *(LAS v2u*)(L + L_PS + wl * 16 + (c & 1) * 8);
      last = *(LAS f32x4*)(L + L_PW + wl * 1024 + lane * 16);
      MERGE3(__uint_as_float(mll.x), __uint_as_float(mll.y));
      pre = true;
    }
  }
#undef MERGE3
  if (__builtin_expect(pre, 1)) {
    f32x4 o;
#pragma unroll
    for (int e = 0; e < 4; ++e) o[e] = fmaf(el, last[e], po[e]);
    const int me = (pair & 15) * 16 + (pair >> 4) * 4 + ((jx >> 19) & 3);
    __builtin_amdgcn_raw_buffer_store_b128(v4u{pk2(o[0] * inv, o[1] * inv), tag, pk2(o[2] * inv, o[3] * inv), tag}, rws, WS_PART + me * 1024 + lane * 16, 0, AUX_SC1);
    __builtin_amdgcn_raw_buffer_store_b64(v2u{__float_as_uint(lm), tag}, rws, lane < 2 ? WS_STAT + (me >> 2) * 64 + lane * 32 + (me & 3) * 8 : WS_END, 0, AUX_SC1);
    asm volatile("" ::: "memory");
    lds_st(L + F_PART, 5u);
  }
  if (__builtin_expect(type == 2, 0)) return;

  const int lslot = ne + nkv - (ne + nkv >= 2 * NSLOT ? 2 * NSLOT : NSLOT);
  if (__builtin_expect(type == 0, 1)) {
    v4u d[4], sa, sb;
    const int doff = WS_PART + (tk * 16 + w * 4) * 1024 + lane * 16, soff = WS_STAT + (tk * 4 + w) * 64 + (c & 1) * 32;
    lds_spin_ge(L + F_LAND, 2 * (ne + nkv + 4) - EARLY_GATHER);
#pragma unroll
    for (int k = 0; k < 4; ++k) d[k] = __builtin_amdgcn_raw_buffer_load_b128(rws, doff, k * 1024, AUX_SC1);
    sa = __builtin_amdgcn_raw_buffer_load_b128(rws, soff, 0, AUX_SC1); sb = __builtin_amdgcn_raw_buffer_load_b128(rws, soff + 16, 0, AUX_SC1);
    __builtin_amdgcn_sched_barrier(0);
    lds_spin_ge(L + F_LAND, 2 * (ne + nkv + 4));
    v2u wf[16];
#pragma unroll
    for (int j = 0; j < 4; ++j) {
      const int so = (lslot + j >= NSLOT ? lslot + j - NSLOT : lslot + j) * SLAB + wtro;
#pragma unroll
      for (int k = 0; k < 4; ++k) wf[4 * j + k] = tr_read(L + so + k * 2048);
    }
    lds_t ow = L + L_AH + w * AH_STR + aperm((c & 1) * 128 + (c >> 1) * 16 + r * 4);
    lds_t ap = L + L_AH + p4 * AH_STR + r * 16;
    asm volatile("" : "+v"(ow), "+v"(ap));
    __builtin_amdgcn_sched_barrier(0);
    for (unsigned spin = 0; spin < SPIN_CAP; ++spin) {
      const unsigned long long b0 = BADLANES(d[0]), b1 = BADLANES(d[1]), b2 = BADLANES(d[2]), b3 = BADLANES(d[3]), bs = BADLANES(sa) | BADLANES(sb);
      if (__builtin_expect((b0 | b1 | b2 | b3 | bs) == 0ull, 1)) break;
      __builtin_amdgcn_s_sleep(2);
      asm volatile("" ::: "memory");
      if (b0) d[0] = __builtin_amdgcn_raw_buffer_load_b128(rws, doff, 0, AUX_SC1);
      if (b1) d[1] = __builtin_amdgcn_raw_buffer_load_b128(rws, doff, 1024, AUX_SC1);
      if (b2) d[2] = __builtin_amdgcn_raw_buffer_load_b128(rws, doff, 2048, AUX_SC1);
      if (b3) d[3] = __builtin_amdgcn_raw_buffer_load_b128(rws, doff, 3072, AUX_SC1);
      sa = __builtin_amdgcn_raw_buffer_load_b128(rws, soff, 0, AUX_SC1); sb = __builtin_amdgcn_raw_buffer_load_b128(rws, soff + 16, 0, AUX_SC1);
    }
    const float la[4] = {__uint_as_float(sa.x), __uint_as_float(sa.z), __uint_as_float(sb.x), __uint_as_float(sb.z)};
    const float M = fmaxf(fmaxf(la[0], la[1]), fmaxf(la[2], la[3]));
    float e[4];
#pragma unroll
    for (int k = 0; k < 4; ++k) e[k] = __builtin_amdgcn_exp2f(la[k] - M);
    const float inv = __builtin_amdgcn_rcpf((e[0] + e[1]) + (e[2] + e[3]));
    f32x4 o = e[0] * f32x4{blo(d[0].x), bhi(d[0].x), blo(d[0].z), bhi(d[0].z)};
#pragma unroll
    for (int k = 1; k < 4; ++k) o += e[k] * f32x4{blo(d[k].x), bhi(d[k].x), blo(d[k].z), bhi(d[k].z)};
    o *= inv;
    *(LAS v2u*)ow = v2u{pk2(o[0], o[1]), pk2(o[2], o[3])};
    asm volatile("s_waitcnt lgkmcnt(0)" ::: "memory");
    __builtin_amdgcn_s_barrier();
    asm volatile("" ::: "memory");
    v4u of[8];
#pragma unroll
    for (int j = 0; j < 8; ++j) of[j] = *(LAS v4u*)(ap + j * 64);
    __builtin_amdgcn_sched_barrier(0);
    f32x4 y = {0.f, 0.f, 0.f, 0.f}, y2 = {0.f, 0.f, 0.f, 0.f};
#pragma unroll
    for (int j = 0; j < 8; j += 2) {
      y = mfma32(v4u{wf[2 * j].x, wf[2 * j].y, wf[2 * j + 1].x, wf[2 * j + 1].y}, of[j], y);
      y2 = mfma32(v4u{wf[2 * j + 2].x, wf[2 * j + 2].y, wf[2 * j + 3].x, wf[2 * j + 3].y}, of[j + 1], y2);
    }
    y += y2;
    PUBLISH_F32(WS_SP + tile * 2048, y);
  } else {
    lds_wait_ge(L + F_H, 1);
    f32x4 y = {0.f, 0.f, 0.f, 0.f};
    lds_t ap = L + L_AH + p4 * AH_STR + r * 16;
    int slot = lslot;
#pragma unroll 1
    for (int j = 0; j < 6; ++j) {
      lds_spin_ge(L + F_LAND, 2 * (ne + nkv + j) + 2);
      GEMV_SLAB(slot * SLAB, ap + j * 128, y);
      slot = slot + 1 == NSLOT ? 0 : slot + 1;
    }
    PUBLISH_F32(WS_FP + tile * 2048, y);
  }
#undef PUBLISH_F32
#undef GEMV_SLAB
  if (__builtin_expect(type == 0 && w == 0, 1)) {
    const int p = lane >> 3, lo16 = ob * 512 + (tk & 3) * 128 + (lane & 7) * 16;
    v4u s0, s1, f0, f1;
    const int o0 = (tcg * 16 + p) * 2048 + lo16, o1 = p < 4 ? o0 + 8 * 2048 : WS_END;
    for (unsigned spin = 0; spin < SPIN_CAP; ++spin) {
      asm volatile("" ::: "memory");
      s0 = __builtin_amdgcn_raw_buffer_load_b128(rws, o0, WS_SP, AUX_SC1);
      s1 = __builtin_amdgcn_raw_buffer_load_b128(rws, o0, WS_SP + 8 * 2048, AUX_SC1);
      f0 = __builtin_amdgcn_raw_buffer_load_b128(rws, o0, WS_FP, AUX_SC1);
      f1 = __builtin_amdgcn_raw_buffer_load_b128(rws, o1, WS_FP, AUX_SC1);
      if ((BADLANES(s0) | BADLANES(s1) | BADLANES(f0) | (BADLANES(f1) & 0xffffffffull)) == 0ull) break;
    }
    float sa = __uint_as_float(s0.x) + __uint_as_float(s1.x), sb = __uint_as_float(s0.z) + __uint_as_float(s1.z);
    float fa = __uint_as_float(f0.x) + __uint_as_float(f1.x), fb = __uint_as_float(f0.z) + __uint_as_float(f1.z);
    sa += dpp<ROR8>(sa); sb += dpp<ROR8>(sb); fa += dpp<ROR8>(fa); fb += dpp<ROR8>(fb);
    sa = rbf(xrow_sum(sa)); sb = rbf(xrow_sum(sb)); fa = rbf(xrow_sum(fa)); fb = rbf(xrow_sum(fb));
    const float ga = rbf(fa * __builtin_amdgcn_rcpf(1.f + __builtin_amdgcn_exp2f(-1.4426950408889634f * fa))), gb = rbf(fb * __builtin_amdgcn_rcpf(1.f + __builtin_amdgcn_exp2f(-1.4426950408889634f * fb)));
    const unsigned res = pk2(blo(xres) + rbf(sa * ga), bhi(xres) + rbf(sb * gb));
    if (lane < 8) *(unsigned*)(out + ob * DM + ocol) = res;
    if (tile == 0 && lane < 32) __builtin_amdgcn_raw_buffer_store_b32(lds_ld(L + L_EP + lane * 4) + 1u, rws, WS_EPOCH + lane * 4, 0, AUX_SC1);
  }
  return;
  }

  if (w == 5) {
    __builtin_amdgcn_s_setprio(3);
    v4u xv[5]; unsigned ep;
#define MOV4(k, a) asm volatile("v_mov_b32 %0, v" #a : "=v"(k))
    asm volatile("s_waitcnt vmcnt(0)" ::: "memory");
    MOV4(xv[0].x, 220); MOV4(xv[0].y, 221); MOV4(xv[0].z, 222); MOV4(xv[0].w, 223); MOV4(xv[1].x, 224); MOV4(xv[1].y, 225); MOV4(xv[1].z, 226); MOV4(xv[1].w, 227);
    MOV4(xv[2].x, 228); MOV4(xv[2].y, 229); MOV4(xv[2].z, 230); MOV4(xv[2].w, 231); MOV4(xv[3].x, 232); MOV4(xv[3].y, 233); MOV4(xv[3].z, 234); MOV4(xv[3].w, 235);
    MOV4(xv[4].x, 236); MOV4(xv[4].y, 237); MOV4(xv[4].z, 238); MOV4(xv[4].w, 239); MOV4(ep, 240);
#undef MOV4
    {
      float s = 0.f;
#pragma unroll
      for (int i = 0; i < 5; ++i)
#pragma unroll
        for (int k = 0; k < 4; ++k) s += blo(xv[i][k]) + bhi(xv[i][k]);
      const float mean = row_sum(s) * (1.f / DM);
      float q = 0.f, q4 = 0.f;
#pragma unroll
      for (int i = 0; i < 5; ++i)
#pragma unroll
        for (int k = 0; k < 4; ++k) {
          const float a = blo(xv[i][k]) - mean, b = bhi(xv[i][k]) - mean;
          if (i < 4) q = fmaf(a, a, fmaf(b, b, q)); else q4 = fmaf(a, a, fmaf(b, b, q4));
        }
      q += c < 8 ? q4 : 0.f;
      const float var = rbf(row_sum(q) * (1.f / DM)), mb = rbf(mean);
      const float inv = __builtin_amdgcn_rcpf(rbf(__builtin_amdgcn_sqrtf(rbf(var + 1e-5f))));
      lds_t ax = L + L_AX + r * AX_STR + (c >> 2) * 64 + (c & 1) * 32 + ((c >> 1) & 1) * 8;
#pragma unroll
      for (int i = 0; i < 5; ++i) {
        unsigned o[4];
#pragma unroll
        for (int k = 0; k < 4; ++k) {
          const unsigned d = pk2(blo(xv[i][k]) - mb, bhi(xv[i][k]) - mb);
          o[k] = pk2(blo(d) * inv, bhi(d) * inv);
        }
        if (i < 4 || c < 8) { *(LAS v2u*)(ax + i * 256) = v2u{o[0], o[1]}; *(LAS v2u*)(ax + i * 256 + 16) = v2u{o[2], o[3]}; }
      }
    }
    unsigned tag = ep;
    tag += dppu<ROR8>(tag); tag += dppu<ROR4>(tag); tag += dppu<ROR2>(tag); tag += dppu<ROR1>(tag);
    tag = (unsigned)__builtin_amdgcn_readlane(tag, 0) + (unsigned)__builtin_amdgcn_readlane(tag, 16) + 0x9E3779B9u;
    tag |= 1u;
    if (lane < 32) lds_st(L + L_EP + lane * 4, ep);
    if (lane == 0) lds_st(L + F_TAG, tag);
    asm volatile("s_waitcnt lgkmcnt(0)" ::: "memory");
    if (lane == 0) lds_st(L + F_XN, 1u);
    while (__builtin_amdgcn_s_memrealtime() - t0 < Q_DELAY) __builtin_amdgcn_s_sleep(2);
    {
      int o[4];
      v4u g[4];
#pragma unroll
      for (int k = 0; k < 4; ++k) { o[k] = WS_QP + ((pair & 15) * 4 + r) * 4096 + (k >> 1) * 2048 + (pair >> 4) * 512 + c * 32 + (k & 1) * 16; g[k] = v4u{0u, 0u, 0u, 0u}; }
      for (unsigned spin = 0; spin < SPIN_CAP; ++spin) {
        asm volatile("" ::: "memory");
        v4u a[4];
#pragma unroll
        for (int k = 0; k < 4; ++k) a[k] = __builtin_amdgcn_raw_buffer_load_b128(rws, o[k], 0, AUX_SC1);
        bool done = true;
#pragma unroll
        for (int k = 0; k < 4; ++k) { const bool ok = TAGOK(a[k]); g[k] = ok ? a[k] : g[k]; o[k] = ok ? WS_END : o[k]; done = done & (o[k] == WS_END); }
        if (__all(done)) break;
      }
      *(LAS v2u*)(L + L_Q + lane * 8) = v2u{pk2(__uint_as_float(g[0].x) + __uint_as_float(g[2].x), __uint_as_float(g[0].z) + __uint_as_float(g[2].z)),
                                            pk2(__uint_as_float(g[1].x) + __uint_as_float(g[3].x), __uint_as_float(g[1].z) + __uint_as_float(g[3].z))};
      asm volatile("s_waitcnt lgkmcnt(0)" ::: "memory");
      if (lane == 0) lds_st(L + F_Q, 1u);
    }
    if (type == 1) {
      const int off = WS_GU + tk * 6 * 1024 + lane * 16;
      lds_t ah = L + L_AH + (c >> 2) * AH_STR + aperm(r * 16 + (c & 3) * 4);
      v4u g[3], u[3], gn[3], un[3];
#pragma unroll
      for (int e = 0; e < 3; ++e) { gn[e] = __builtin_amdgcn_raw_buffer_load_b128(rws, off, e * 1024, AUX_SC1); un[e] = __builtin_amdgcn_raw_buffer_load_b128(rws, off, (72 + e) * 1024, AUX_SC1); }
      unsigned spin = 0;
#pragma unroll 1
      for (int j = 0; j < 6;) {
#pragma unroll
        for (int e = 0; e < 3; ++e) { g[e] = gn[e]; u[e] = un[e]; }
        const bool ok = (BADLANES(g[0]) | BADLANES(u[0]) | BADLANES(g[1]) | BADLANES(u[1]) | BADLANES(g[2]) | BADLANES(u[2])) == 0ull || ++spin >= SPIN_CAP;
        const int jn = ok ? j + 3 : j;
        asm volatile("" ::: "memory");
        if (jn < 6) {
#pragma unroll
          for (int e = 0; e < 3; ++e) { gn[e] = __builtin_amdgcn_raw_buffer_load_b128(rws, off + jn * 1024, e * 1024, AUX_SC1); un[e] = __builtin_amdgcn_raw_buffer_load_b128(rws, off + jn * 1024, (72 + e) * 1024, AUX_SC1); }
        }
        if (ok) {
#pragma unroll
          for (int e = 0; e < 3; ++e) {
            const float gv[4] = {blo(g[e].x), bhi(g[e].x), blo(g[e].z), bhi(g[e].z)}, uv[4] = {blo(u[e].x), bhi(u[e].x), blo(u[e].z), bhi(u[e].z)};
            float hv[4];
#pragma unroll
            for (int i = 0; i < 4; ++i) {
              const float a = gv[i], inner = 0.7978845608028654f * fmaf(0.044715f * a * a, a, a);
              hv[i] = rbf(a * __builtin_amdgcn_rcpf(1.f + __builtin_amdgcn_exp2f(-2.8853900817779268f * inner))) * uv[i];
            }
            *(LAS v2u*)(ah + (j + e) * 128) = v2u{pk2(hv[0], hv[1]), pk2(hv[2], hv[3])};
          }
        }
        j = jn;
      }
      asm volatile("s_waitcnt lgkmcnt(0)" ::: "memory");
      if (lane == 0) lds_st(L + F_H, 1u);
    }
    return;
  }

  {
    __builtin_amdgcn_s_setprio(3);
    {
    const int stride = nq ? QD * 2 : FF * 2;
    const rsrc_t rw = mk_rsrc(nq ? wq + (qj >> 1) * 64 : gubase, 575 * stride + 128);
    const int voff = prow * stride + pch * 16, so0 = nq ? qs0 * 64 * stride : 0;
#pragma unroll 1
    for (int s = 0; s < n0; ++s) dma_wslab(rw, voff, so0 + s * 64 * stride, 8 * stride, ring + s * SLAB);
    }
    int s = n0, pub = 0;
#define PUB(v) do { pub = max(pub, (v)); lds_st(L + F_LAND, pub); } while (0)
    {
      const rsrc_t rw = mk_rsrc(gubase, gj < 0 ? 0 : 575 * (FF * 2) + 128);
      const int voff = prow * (FF * 2) + pch * 16;
#pragma unroll 1
      for (; s < ne; ++s) {
        wait_vm<8 * (WS_E - 1)>();
        PUB(2 * (s - (WS_E - 1)));
        dma_wslab(rw, voff, (s - nq) * 64 * (FF * 2), 8 * (FF * 2), ring + s * SLAB);
      }
    }
    int slot = ne;
#define RINGSPACE(last) do { \
      if ((last) - NSLOT >= freed) { \
        bool drained = false; \
        for (;;) { \
          asm volatile("" ::: "memory"); \
          const v4u cv = *(volatile LAS v4u*)(L + F_CONS); \
          freed = __builtin_amdgcn_readfirstlane(min(min(cv.x, cv.y), min(cv.z, cv.w))); \
          if ((last) - NSLOT < freed) break; \
          if (!drained) { wait_vm<0>(); PUB(2 * s); drained = true; } \
          __builtin_amdgcn_s_sleep(1); \
        } \
      } } while (0)
    int freed = 0;
    {
      const size_t kvbase = ((size_t)pair * SK + (SK - WIN) + ((jx >> 6) & 127) * 16) * DH;
      const rsrc_t rk = mk_rsrc(Kc + kvbase, nkv * 4096), rv = mk_rsrc(Vc + kvbase, nkv * 4096);
      const unsigned tk0 = (unsigned)__builtin_amdgcn_s_memrealtime();
      const int dt64 = ((type == 1 ? T_NOM1 : T_NOM) - (int)(tk0 - (unsigned)t0)) * 64 / nkv;
      int vk[4], vv[4];
#pragma unroll
      for (int p = 0; p < 4; ++p) { vk[p] = p * 1024 + r * 256 + ((c ^ (4 * p + r)) << 4); vv[p] = p * 1024 + r * 256 + ((c ^ (((4 * p + r) & 7) << 1)) << 4); }
      unsigned now = tk0;
      bool wide = false;
#pragma unroll 1
      for (int i = 0; i < nkv; ++i, ++s) {
        const int lag = (int)(now - tk0) * 64 - (i - 1) * dt64;
        const bool boost = lag > LAG_HI * 64 && i >= BOOST_FROM;
        if (boost || wide) { wait_vm<44>(); PUB(2 * s - 11); wait_vm<40>(); PUB(2 * s - 10); }
        if (!boost) {
          if (wide) { wait_vm<36>(); PUB(2 * s - 9); wait_vm<32>(); PUB(2 * s - 8); }
          wait_vm<28>(); PUB(2 * s - 7); wait_vm<24>(); PUB(2 * s - 6);
          if (lag < -LEAD_MAX * 64) {
            wait_vm<20>(); PUB(2 * s - 5); wait_vm<16>(); PUB(2 * s - 4);
            while ((int)((unsigned)__builtin_amdgcn_s_memrealtime() - tk0) * 64 - i * dt64 < -LEAD_MAX * 64) __builtin_amdgcn_s_sleep(1);
          }
        }
        wide = boost;
        RINGSPACE(s);
        now = (unsigned)__builtin_amdgcn_s_memrealtime();
        dma_kvslab(rk, rv, vk, vv, i * 4096, ring + slot * SLAB);
        slot = slot + 1 == NSLOT ? 0 : slot + 1;
      }
    }
#define LADDER() do { \
    wait_vm<44>(); PUB(2 * s - 11); wait_vm<40>(); PUB(2 * s - 10); \
    wait_vm<36>(); PUB(2 * s - 9); wait_vm<32>(); PUB(2 * s - 8); \
    wait_vm<28>(); PUB(2 * s - 7); wait_vm<24>(); PUB(2 * s - 6); \
    wait_vm<20>(); PUB(2 * s - 5); wait_vm<16>(); PUB(2 * s - 4); \
    wait_vm<12>(); PUB(2 * s - 3); wait_vm<8>(); PUB(2 * s - 2); \
    wait_vm<4>(); PUB(2 * s - 1); wait_vm<0>(); PUB(2 * s); } while (0)
    const rsrc_t rl = mk_rsrc(type == 0 ? wo + tk * 256 * DM + tcg * 64 : wdn + tk * 384 * DM + tcg * 64, (nl * 64 - 1) * (DM * 2) + 128);
    const int lvoff = prow * (DM * 2) + pch * 16;
    if (nl) RINGSPACE(s + nl - 1);
    LADDER();
#pragma unroll 1
    for (int i = 0; i < nl; ++i, ++s) {
      if (i == LATE_FIRST) lds_spin_ge(L + F_PART, 5);
      dma_wslab(rl, lvoff, i * 64 * (DM * 2), 8 * (DM * 2), ring + slot * SLAB);
      slot = slot + 1 == NSLOT ? 0 : slot + 1;
    }
    LADDER();
#undef LADDER
#undef RINGSPACE
#undef PUB
  }
}

constexpr int PEN3 = 70;
constexpr int LATE_COST = 10;
constexpr int ODDPEN = 5;
static void build_jobs(Jobs& J) {
  int wgs[3][256], nw[3] = {0, 0, 0};
  int type[256], tile[256], qj[256], gj[256], cost[256];
  for (int wg = 0; wg < 256; ++wg) {
    const int xcd = wg & 7, idx = wg >> 3, gi = idx & 15, o = (idx >> 4) * 7 + gi - 9;
    const int j = (xcd & 1) * 5 + o - 9;
    type[wg] = gi < 9 ? 0 : (o < 9 || j < 9) ? 1 : 2;
    tile[wg] = gi < 9 ? gi * 16 + xcd * 2 + (idx >> 4) : o < 9 ? o * 16 + xcd : j < 9 ? j * 16 + 8 + (xcd >> 1) : 0;
    wgs[type[wg]][nw[type[wg]]++] = wg;
    qj[wg] = gj[wg] = -1;
  }
  int fast[126], nf = 0, slow[18], nsl = 0, halves[128], nh = 0;
  for (int g = 0; g < 144; ++g) { if ((g & 7) == 3) slow[nsl++] = g; else fast[nf++] = g; }
  for (int h = 0; h < 128; ++h) if (((h >> 1) & 7) == 3) halves[nh++] = h;
  for (int h = 0; h < 128; ++h) if (((h >> 1) & 7) != 3 && !(h & 1)) halves[nh++] = h;
  for (int h = 0; h < 128; ++h) if (((h >> 1) & 7) != 3 && (h & 1)) halves[nh++] = h;
  int ih = 0;
  for (int i = 0; i < 4; ++i) gj[wgs[2][i]] = slow[i];
  for (int i = 0; i < 108; ++i) { if (i < 94) qj[wgs[1][i]] = halves[ih++]; else gj[wgs[1][i]] = slow[i - 90]; }
  for (int i = 0; i < 144; ++i) { if (i < 34) qj[wgs[0][i]] = halves[ih++]; if (i >= 18) gj[wgs[0][i]] = fast[i - 18]; }
  for (int wg = 0; wg < 256; ++wg) {
    const int nq = qj[wg] < 0 ? 0 : 5 - (qj[wg] & 1);
    cost[wg] = 10 * nq + (nq && ((qj[wg] >> 1) & 7) == 3 ? PEN3 * nq / 9 : 0) + (gj[wg] < 0 ? 0 : (gj[wg] & 7) == 3 ? 90 + PEN3 : 90) +
               (type[wg] == 1 ? LATE_COST : 0) + ((wg & 1) ? ODDPEN : 0);
  }
  int ord[256];
  for (int i = 0; i < 256; ++i) ord[i] = i;
  for (int i = 1; i < 256; ++i) { const int v = ord[i]; int j = i; while (j > 0 && cost[ord[j - 1]] < cost[v]) { ord[j] = ord[j - 1]; --j; } ord[j] = v; }
  int grp[64][4], sum4[64];
  for (int p = 0; p < 64; ++p) {
    grp[p][0] = ord[p]; grp[p][1] = ord[127 - p]; grp[p][2] = ord[128 + p]; grp[p][3] = ord[255 - p];
    sum4[p] = cost[grp[p][0]] + cost[grp[p][1]] + cost[grp[p][2]] + cost[grp[p][3]];
  }
  for (int pass = 0; pass < 3; ++pass)
    for (int p = 0; p < 64; ++p) for (int q = 0; q < 64; ++q) for (int a = 0; a < 4; ++a) for (int b = 0; b < 4; ++b) {
      const int d = cost[grp[p][a]] - cost[grp[q][b]], gap = sum4[p] - sum4[q];
      if (d > 0 && d < gap) { const int t = grp[p][a]; grp[p][a] = grp[q][b]; grp[q][b] = t; sum4[p] -= d; sum4[q] += d; }
    }
  for (int p = 0; p < 64; ++p) {
    const int* mem = grp[p];
    const int T = (1280 + sum4[p]) / 4;
    int n[4], sum = 0;
    for (int j = 0; j < 4; ++j) { n[j] = (T - cost[mem[j]] + 5) / 10; sum += n[j]; }
    for (int j = 3; sum != 128; j = (j + 3) & 3) { if (sum < 128) { ++n[j]; ++sum; } else { --n[3 - j]; --sum; } }
    int s0 = 0;
    for (int j = 0; j < 4; ++j) {
      const int wg = mem[j];
      J.v[2 * wg] = (unsigned)p | (unsigned)s0 << 6 | (unsigned)n[j] << 13 | (unsigned)j << 19 | (unsigned)type[wg] << 21;
      J.v[2 * wg + 1] = (unsigned)(qj[wg] + 1) | (unsigned)(gj[wg] + 1) << 8 | (unsigned)tile[wg] << 16;
      s0 += n[j];
    }
  }
}
}

extern "C" void kernel_launch(void* const* d_in, const int* in_sizes, int n_in, void* d_out, int out_size, void* d_ws, size_t ws_size, hipStream_t stream) {
  Jobs J;
  build_jobs(J);
  fused_decode<<<256, 384, 0, stream>>>((const u16*)d_in[0], (const u16*)d_in[1], (const u16*)d_in[2], (const u16*)d_in[3], (const u16*)d_in[4],
                                        (const u16*)d_in[6], (const u16*)d_in[7], (const u16*)d_in[8], (u16*)d_out, (unsigned char*)d_ws, J);
}
```

```cpp
#include <hip/hip_runtime.h>
#include <hip/hip_bf16.h>
#include <stdint.h>

#define LAS __attribute__((address_space(3)))
typedef unsigned v4u __attribute__((ext_vector_type(4)));
typedef unsigned v2u __attribute__((ext_vector_type(2)));
typedef float f32x4 __attribute__((ext_vector_type(4)));
typedef short s16x4 __attribute__((ext_vector_type(4)));
typedef __bf16 bf16x2 __attribute__((ext_vector_type(2)));
typedef __bf16 bf16x8 __attribute__((ext_vector_type(8)));
typedef unsigned short u16;
typedef __amdgpu_buffer_rsrc_t rsrc_t;

namespace {
constexpr int DM = 576, SK = 8192, DH = 128, WIN = 2048, FF = 4608, QD = 4096;
constexpr int AUX_SC1 = 16;
constexpr int WS_E = 5;
constexpr int T_NOM = 1180, T_NOM1 = 1160;
constexpr int LAG_HI = 15, LEAD_MAX = 40;
constexpr int BOOST_FROM = 10;
constexpr int LATE_THR = 20;
constexpr int LATE_FIRST = 2;
constexpr int EARLY_GATHER = 2;
constexpr int Q_DELAY = 390;
constexpr int CODE_BYTES = 14064;
constexpr unsigned SPIN_CAP = 30000;

constexpr int WS_EPOCH = 0;
constexpr int WS_GU = 4096;
constexpr int WS_QP = WS_GU + 144 * 1024;
constexpr int WS_PART = WS_QP + 128 * 2048;
constexpr int WS_STAT = WS_PART + 256 * 1024;
constexpr int WS_FP = WS_STAT + 4096;
constexpr int WS_SP = WS_FP + 144 * 2048;
constexpr int WS_END = WS_SP + 144 * 2048;

constexpr int NSLOT = 18, SLAB = 8192;
constexpr int L_AX = NSLOT * SLAB, AX_STR = 1216;
constexpr int L_PW = L_AX;
constexpr int L_AH = L_AX + 4 * AX_STR, AH_STR = 1216;
constexpr int L_Q = L_AH + 4 * AH_STR;
constexpr int L_PS = L_Q + 512;
constexpr int L_EP = L_PS + 64;
constexpr int L_FLAG = L_EP + 128;
constexpr int F_CONS = L_FLAG, F_TAG = L_FLAG + 16, F_Q = L_FLAG + 20, F_LAND = L_FLAG + 24, F_XN = L_FLAG + 28, F_PART = L_FLAG + 32, F_H = L_FLAG + 36;
constexpr int L_BYTES = L_FLAG + 64;
static_assert(L_BYTES <= 160 * 1024 && L_BYTES > 80 * 1024, "one workgroup per CU");

struct Jobs { unsigned v[512]; };

__device__ __forceinline__ rsrc_t mk_rsrc(const void* p, int bytes) { return __builtin_amdgcn_make_buffer_rsrc(const_cast<void*>(p), 0, bytes, 0x00020000); }
__device__ __forceinline__ float blo(unsigned u) { return __uint_as_float(u << 16); }
__device__ __forceinline__ float bhi(unsigned u) { return __uint_as_float(u & 0xffff0000u); }
__device__ __forceinline__ unsigned pk2(float lo, float hi) {
  bf16x2 r; r[0] = (__bf16)lo; r[1] = (__bf16)hi; return __builtin_bit_cast(unsigned, r);
}
__device__ __forceinline__ float rbf(float f) { return (float)(__bf16)f; }
template <int CTRL> __device__ __forceinline__ float dpp(float x) {
  return __builtin_bit_cast(float, __builtin_amdgcn_mov_dpp(__builtin_bit_cast(int, x), CTRL, 0xf, 0xf, true));
}
template <int CTRL> __device__ __forceinline__ unsigned dppu(unsigned x) { return (unsigned)__builtin_amdgcn_mov_dpp((int)x, CTRL, 0xf, 0xf, true); }
constexpr int ROR8 = 0x128, ROR4 = 0x124, ROR2 = 0x122, ROR1 = 0x121;
__device__ __forceinline__ float xrow_max(float x) {
  auto s = __builtin_amdgcn_permlane16_swap(__float_as_uint(x), __float_as_uint(x), false, false);
  x = fmaxf(__uint_as_float(s[0]), __uint_as_float(s[1]));
  auto t = __builtin_amdgcn_permlane32_swap(__float_as_uint(x), __float_as_uint(x), false, false);
  return fmaxf(__uint_as_float(t[0]), __uint_as_float(t[1]));
}
__device__ __forceinline__ float xrow_sum(float x) {
  auto s = __builtin_amdgcn_permlane16_swap(__float_as_uint(x), __float_as_uint(x), false, false);
  x = __uint_as_float(s[0]) + __uint_as_float(s[1]);
  auto t = __builtin_amdgcn_permlane32_swap(__float_as_uint(x), __float_as_uint(x), false, false);
  return __uint_as_float(t[0]) + __uint_as_float(t[1]);
}
__device__ __forceinline__ float row_sum(float x) { x += dpp<ROR8>(x); x += dpp<ROR4>(x); x += dpp<ROR2>(x); x += dpp<ROR1>(x); return x; }
__device__ __forceinline__ f32x4 mfma32(v4u a, v4u b, f32x4 c) {
  return __builtin_amdgcn_mfma_f32_16x16x32_bf16(__builtin_bit_cast(bf16x8, a), __builtin_bit_cast(bf16x8, b), c, 0, 0, 0);
}
__device__ __forceinline__ f32x4 mfma16(v2u a, v2u b, f32x4 c) {
  return __builtin_amdgcn_mfma_f32_16x16x16bf16_1k(__builtin_bit_cast(s16x4, a), __builtin_bit_cast(s16x4, b), c, 0, 0, 0);
}
#define TAGOK(v) ((((v).y ^ tag) | ((v).w ^ tag)) == 0u)
#define BADLANES(v) (__builtin_amdgcn_ballot_w64((v).y != tag) | __builtin_amdgcn_ballot_w64((v).w != tag))

typedef LAS unsigned char* lds_t;
__device__ __forceinline__ v2u tr_read(lds_t p) { return __builtin_bit_cast(v2u, __builtin_amdgcn_ds_read_tr16_b64_v4i16((LAS s16x4*)p)); }
__device__ __forceinline__ unsigned lds_ld(lds_t p) { return __hip_atomic_load((LAS unsigned*)p, __ATOMIC_RELAXED, __HIP_MEMORY_SCOPE_WORKGROUP); }
__device__ __forceinline__ void lds_st(lds_t p, unsigned v) { __hip_atomic_store((LAS unsigned*)p, v, __ATOMIC_RELAXED, __HIP_MEMORY_SCOPE_WORKGROUP); }
__device__ __forceinline__ void lds_wait_ge(lds_t p, unsigned want) {
  while ((unsigned)__builtin_amdgcn_readfirstlane(lds_ld(p)) < want) __builtin_amdgcn_s_sleep(1);
  asm volatile("" ::: "memory");
}
__device__ __forceinline__ void lds_spin_ge(lds_t p, unsigned want) {
  unsigned v;
  do v = __builtin_amdgcn_readfirstlane(lds_ld(p)); while (v < want);
  asm volatile("" ::: "memory");
}
template <int N> __device__ __forceinline__ void wait_vm() { asm volatile("s_waitcnt vmcnt(%0)" ::"i"(N) : "memory"); }
#define DMA_OPS " offen nt lds\n\t"
__device__ __forceinline__ void dma_wslab(rsrc_t r, int voff, int soff, int step, unsigned ldsoff) {
  int so;
#define NEXT "s_add_u32 m0, m0, 0x400\n\ts_add_u32 %0, %0, %4\n\tbuffer_load_dwordx4 %2, %3, %0" DMA_OPS
  asm volatile("s_mov_b32 m0, %1\n\ts_mov_b32 %0, %5\n\tbuffer_load_dwordx4 %2, %3, %0" DMA_OPS NEXT NEXT NEXT NEXT NEXT NEXT NEXT
               : "=&s"(so) : "s"(ldsoff), "v"(voff), "s"(r), "s"(step), "s"(soff) : "memory", "scc");
#undef NEXT
}
__device__ __forceinline__ void dma_kvslab(rsrc_t rk, rsrc_t rv, const int (&vk)[4], const int (&vv)[4], int soff, unsigned ldsoff) {
#define NEXT(V, R) "s_add_u32 m0, m0, 0x400\n\ts_nop 0\n\tbuffer_load_dwordx4 " V ", " R ", %0" DMA_OPS
  asm volatile("s_mov_b32 m0, %1\n\ts_nop 0\n\tbuffer_load_dwordx4 %4, %2, %0" DMA_OPS NEXT("%5", "%2") NEXT("%6", "%2") NEXT("%7", "%2")
               NEXT("%8", "%3") NEXT("%9", "%3") NEXT("%10", "%3") NEXT("%11", "%3")
               :: "s"(soff), "s"(ldsoff), "s"(rk), "s"(rv), "v"(vk[0]), "v"(vk[1]), "v"(vk[2]), "v"(vk[3]), "v"(vv[0]), "v"(vv[1]), "v"(vv[2]), "v"(vv[3]) : "memory", "scc");
#undef NEXT
}
__device__ __forceinline__ int aperm(int k) { const int g = (k >> 2) & 7; return (k >> 5) * 64 + (g & 3) * 16 + (g >> 2) * 8; }

__global__ void __launch_bounds__(384, 2)
fused_decode(const u16* __restrict__ x, const u16* __restrict__ Kc, const u16* __restrict__ Vc, const u16* __restrict__ wq, const u16* __restrict__ wo,
             const u16* __restrict__ wgate, const u16* __restrict__ wup, const u16* __restrict__ wdn, u16* __restrict__ out, unsigned char* __restrict__ ws,
             const Jobs jobs) {
  __shared__ __attribute__((aligned(16))) unsigned char smem[L_BYTES];
  lds_t L = (lds_t)smem;
  unsigned long long pc0;
  asm volatile("s_getpc_b64 %0" : "=s"(pc0));
  const int lane = threadIdx.x & 63, w = __builtin_amdgcn_readfirstlane(threadIdx.x >> 6), wg = blockIdx.x;
  asm volatile("buffer_load_dword v241, %0, %1, 0 offen" ::"v"(threadIdx.x < 64 ? (wg >> 3) * 1024 + lane * 16 : CODE_BYTES),
               "s"(mk_rsrc((const void*)(pc0 & ~255ull), CODE_BYTES)) : "memory", "v241");
  lds_st(L + L_FLAG + (lane & 15) * 4, 0u);
  const unsigned jx = jobs.v[2 * wg], jy = jobs.v[2 * wg + 1];
  asm volatile("" ::"s"(x), "s"(Kc), "s"(Vc), "s"(wq), "s"(wo), "s"(wgate), "s"(wup), "s"(wdn), "s"(out), "s"(ws), "s"(jx), "s"(jy) : "memory");
  asm volatile("s_waitcnt lgkmcnt(0)" ::: "memory");
  const int pair = jx & 63, nkv = (jx >> 13) & 63, type = (jx >> 21) & 3, qj = (int)(jy & 255) - 1, gj = (int)((jy >> 8) & 255) - 1, tile = jy >> 16;
  const int nq = qj < 0 ? 0 : 5 - (qj & 1), qs0 = (qj & 1) * 5, ne = nq + (gj < 0 ? 0 : 9), nl = type == 0 ? 4 : type == 1 ? 6 : 0;
  const int tk = tile & 15, tcg = tile >> 4;
  const rsrc_t rws = mk_rsrc(ws, WS_END);
  const int r = lane >> 4, c = lane & 15;
  if (w == 5) {
    const rsrc_t rx = mk_rsrc(x, 4 * DM * 2);
    const int xo = r * (DM * 2) + c * 16;
    asm volatile("buffer_load_dwordx4 v[220:223], %0, %3, 0 offen\n\tbuffer_load_dwordx4 v[224:227], %0, %3, 0 offen offset:256\n\t"
                 "buffer_load_dwordx4 v[228:231], %0, %3, 0 offen offset:512\n\tbuffer_load_dwordx4 v[232:235], %0, %3, 0 offen offset:768\n\t"
                 "buffer_load_dwordx4 v[236:239], %1, %3, 0 offen\n\tbuffer_load_dword v240, %2, %4, 0 offen sc1"
                 ::"v"(xo), "v"(c < 8 ? xo + 1024 : 0x10000), "v"(WS_EPOCH + (lane & 31) * 4), "s"(rx), "s"(rws)
                 : "memory", "v220", "v221", "v222", "v223", "v224", "v225", "v226", "v227", "v228", "v229", "v230", "v231", "v232", "v233", "v234", "v235",
                   "v236", "v237", "v238", "v239", "v240");
  }
  const unsigned long long t0 = __builtin_amdgcn_s_memrealtime();
  __builtin_amdgcn_s_barrier();
  asm volatile("" ::: "memory");
  const int prow = lane >> 3, pch = (lane & 7) ^ (((prow >> 1) & 3) << 1);
  const unsigned ring = (unsigned)(size_t)L;
  const u16* gubase = gj >= 72 ? wup + (gj - 72) * 64 : wgate + gj * 64;
  const int n0 = nq ? nq : WS_E;
  if (w < 4) {
  unsigned xres = 0;
  const int ob = tk >> 2, ocol = tcg * 64 + (tk & 3) * 16 + (lane & 7) * 2;
  if (w == 0 && type == 0) xres = *(const unsigned*)(x + ob * DM + ocol);
  const int q4 = (lane >> 2) & 3, p4 = lane & 3;
  const int wtro = (4 * r + q4) * 128 + (((2 * w + (p4 >> 1)) ^ (((2 * r + (q4 >> 1)) & 3) << 1)) << 4) + (p4 & 1) * 8;
#define GEMV_SLAB(slotoff, ap, acc) do { \
    const v2u l0_ = tr_read(L + (slotoff) + wtro), h0_ = tr_read(L + (slotoff) + wtro + 2048), l1_ = tr_read(L + (slotoff) + wtro + 4096), h1_ = tr_read(L + (slotoff) + wtro + 6144); \
    const v4u b0_ = *(LAS v4u*)(ap), b1_ = *(LAS v4u*)((ap) + 64); \
    acc = mfma32(v4u{l0_.x, l0_.y, h0_.x, h0_.y}, b0_, acc); acc = mfma32(v4u{l1_.x, l1_.y, h1_.x, h1_.y}, b1_, acc); } while (0)
  lds_wait_ge(L + F_XN, 1);
  const unsigned tag = lds_ld(L + F_TAG);
  const int x4 = c >> 2, f32off = x4 < 2 ? (c & 3) * 512 + (8 * w + 2 * r + x4) * 16 : WS_END;
#define PUBLISH_F32(dst, y) __builtin_amdgcn_raw_buffer_store_b128(v4u{__float_as_uint((x4 & 1) ? y[2] : y[0]), tag, __float_as_uint((x4 & 1) ? y[3] : y[1]), tag}, rws, f32off, (dst), AUX_SC1)
  if (__builtin_expect(nq != 0, 1)) {
    f32x4 y = {0.f, 0.f, 0.f, 0.f};
    lds_t ap = L + L_AX + p4 * AX_STR + r * 16 + qs0 * 128;
#pragma unroll 1
    for (int j = 0; j < nq; ++j) {
      lds_spin_ge(L + F_LAND, 2 * j + 2);
      GEMV_SLAB(j * SLAB, ap + j * 128, y);
    }
    PUBLISH_F32(WS_QP + qj * 2048, y);
  }
  if (__builtin_expect(gj >= 0, 1)) {
    f32x4 y = {0.f, 0.f, 0.f, 0.f};
    lds_t ap = L + L_AX + p4 * AX_STR + r * 16;
#pragma unroll 1
    for (int j = 0; j < 9; ++j) {
      lds_spin_ge(L + F_LAND, 2 * (nq + j) + 2);
      GEMV_SLAB((nq + j) * SLAB, ap + j * 128, y);
    }
    __builtin_amdgcn_raw_buffer_store_b128(v4u{pk2(y[0], y[1]), tag, pk2(y[2], y[3]), tag}, rws, c < 4 ? WS_GU + gj * 1024 + w * 256 + c * 64 + r * 16 : WS_END, 0, AUX_SC1);
  }
  asm volatile("" ::: "memory");
  lds_st(L + F_CONS + w * 4, ne + w);
  lds_wait_ge(L + F_Q, 1);
  v4u qb[4];
#pragma unroll
  for (int s = 0; s < 4; ++s) qb[s] = *(LAS v4u*)(L + L_Q + (c & 1) * 256 + s * 64 + r * 16);
  int kro[4], vro[8];
#pragma unroll
  for (int s = 0; s < 4; ++s) kro[s] = c * 256 + (((4 * s + r) ^ c) << 4);
#pragma unroll
  for (int t = 0; t < 8; ++t) vro[t] = 4096 + (4 * r + q4) * 256 + (((2 * t + (p4 >> 1)) ^ (((4 * r + q4) & 7) << 1)) << 4) + (p4 & 1) * 8;
  constexpr float C2 = 0.08838834764831845f * 1.4426950408889634f;
  float m = -INFINITY, l = 0.f;
  const int wl = (nkv - 1) & 3;
  f32x4 po = {0.f, 0.f, 0.f, 0.f};
  float el = 0.f, inv = 0.f, lm = 0.f;
  bool pre = false;
#define MERGE3(mlast, llast) do { \
    v2u ml_[3]; f32x4 pw_[3]; \
    _Pragma("unroll") for (int k = 0; k < 3; ++k) { const int wk_ = (wl + 1 + k) & 3; \
      ml_[k] = *(LAS v2u*)(L + L_PS + wk_ * 16 + (c & 1) * 8); pw_[k] = *(LAS f32x4*)(L + L_PW + wk_ * 1024 + lane * 16); } \
    const float ml3_ = (mlast), M_ = fmaxf(fmaxf(__uint_as_float(ml_[0].x), __uint_as_float(ml_[1].x)), fmaxf(__uint_as_float(ml_[2].x), ml3_)); \
    const float e0_ = __builtin_amdgcn_exp2f(__uint_as_float(ml_[0].x) - M_), e1_ = __builtin_amdgcn_exp2f(__uint_as_float(ml_[1].x) - M_), \
                e2_ = __builtin_amdgcn_exp2f(__uint_as_float(ml_[2].x) - M_); \
    el = __builtin_amdgcn_exp2f(ml3_ - M_); \
    const float Ls_ = fmaf(el, (llast), fmaf(e2_, __uint_as_float(ml_[2].y), fmaf(e1_, __uint_as_float(ml_[1].y), e0_ * __uint_as_float(ml_[0].y)))); \
    _Pragma("unroll") for (int e = 0; e < 4; ++e) po[e] = fmaf(e2_, pw_[2][e], fmaf(e1_, pw_[1][e], e0_ * pw_[0][e])); \
    inv = __builtin_amdgcn_rcpf(Ls_); lm = M_ + __builtin_amdgcn_logf(Ls_); } while (0)
  f32x4 oa = {0.f, 0.f, 0.f, 0.f}, ob2 = {0.f, 0.f, 0.f, 0.f};
  {
    int slot = ne + w;
#pragma unroll 1
    for (int i = w; i < nkv; i += 4) {
      lds_spin_ge(L + F_LAND, 2 * (ne + i) + 1);
      lds_t sb = L + slot * SLAB;
      v4u kf[4]; v2u vf[8];
#pragma unroll
      for (int s = 0; s < 4; ++s) kf[s] = *(LAS v4u*)(sb + kro[s]);
      const f32x4 zero = {0.f, 0.f, 0.f, 0.f};
      const f32x4 z = (mfma32(kf[1], qb[1], mfma32(kf[0], qb[0], zero)) + mfma32(kf[3], qb[3], mfma32(kf[2], qb[2], zero))) * C2;
      const float mn = fmaxf(m, xrow_max(fmaxf(fmaxf(z[0], z[1]), fmaxf(z[2], z[3])))), al = __builtin_amdgcn_exp2f(m - mn);
      m = mn;
      const v2u P = {pk2(__builtin_amdgcn_exp2f(z[0] - m), __builtin_amdgcn_exp2f(z[1] - m)), pk2(__builtin_amdgcn_exp2f(z[2] - m), __builtin_amdgcn_exp2f(z[3] - m))};
      oa *= al; ob2 *= al;
      l = fmaf(l, al, (blo(P.x) + bhi(P.x)) + (blo(P.y) + bhi(P.y)));
      v2u Pt[8];
#pragma unroll
      for (int t = 0; t < 8; ++t) Pt[t] = (c >> 1) == t ? P : v2u{0u, 0u};
      unsigned va[8];
#pragma unroll
      for (int t = 0; t < 8; ++t) { va[t] = (unsigned)(size_t)(sb + vro[t]); asm volatile("" : "+v"(Pt[t]), "+v"(va[t])); }
      asm volatile("" : "+v"(oa), "+v"(ob2), "+v"(l));
      if (i == nkv - 1 && (unsigned)__builtin_amdgcn_readfirstlane(lds_ld(L + F_PART)) == 3u) {
        asm volatile("" ::: "memory");
        MERGE3(m, xrow_sum(l));
        pre = true;
        asm volatile("" : "+v"(po), "+v"(el), "+v"(inv), "+v"(lm));
      }
      lds_spin_ge(L + F_LAND, 2 * (ne + i) + 2);
#pragma unroll
      for (int t = 0; t < 8; ++t) vf[t] = tr_read((lds_t)(size_t)va[t]);
      __builtin_amdgcn_sched_barrier(0);
#pragma unroll
      for (int t = 0; t < 8; t += 2) { oa = mfma16(vf[t], Pt[t], oa); ob2 = mfma16(vf[t + 1], Pt[t + 1], ob2); }
      asm volatile("" ::: "memory");
      lds_st(L + F_CONS + w * 4, ne + min(i + 4, nkv));
      slot = slot + 4 >= NSLOT ? slot + 4 - NSLOT : slot + 4;
    }
  }
  f32x4 last = oa + ob2;
  if (__builtin_expect(!pre, 0)) {
    l = xrow_sum(l);
    *(LAS f32x4*)(L + L_PW + w * 1024 + lane * 16) = last;
    if (lane < 2) *(LAS v2u*)(L + L_PS + w * 16 + lane * 8) = v2u{__float_as_uint(m), __float_as_uint(l)};
    unsigned arrived, t;
    asm volatile("s_mov_b64 exec, 1\n\tds_add_rtn_u32 %1, %2, %3\n\ts_mov_b64 exec, -1\n\ts_waitcnt lgkmcnt(0)\n\tv_readfirstlane_b32 %0, %1"
                 : "=s"(arrived), "=&v"(t) : "v"((unsigned)(size_t)(L + F_PART)), "v"(1u) : "memory");
    if (arrived == 3) {
      const v2u mll = *(LAS v2u*)(L + L_PS + wl * 16 + (c & 1) * 8);
      last = *(LAS f32x4*)(L + L_PW + wl * 1024 + lane * 16);
      MERGE3(__uint_as_float(mll.x), __uint_as_float(mll.y));
      pre = true;
    }
  }
#undef MERGE3
  if (__builtin_expect(pre, 1)) {
    f32x4 o;
#pragma unroll
    for (int e = 0; e < 4; ++e) o[e] = fmaf(el, last[e], po[e]);
    const int me = (pair & 15) * 16 + (pair >> 4) * 4 + ((jx >> 19) & 3);
    __builtin_amdgcn_raw_buffer_store_b128(v4u{pk2(o[0] * inv, o[1] * inv), tag, pk2(o[2] * inv, o[3] * inv), tag}, rws, WS_PART + me * 1024 + lane * 16, 0, AUX_SC1);
    __builtin_amdgcn_raw_buffer_store_b64(v2u{__float_as_uint(lm), tag}, rws, lane < 2 ? WS_STAT + (me >> 2) * 64 + lane * 32 + (me & 3) * 8 : WS_END, 0, AUX_SC1);
    asm volatile("" ::: "memory");
    lds_st(L + F_PART, 5u);
  }
  if (__builtin_expect(type == 2, 0)) return;

  const int lslot = ne + nkv - (ne + nkv >= 2 * NSLOT ? 2 * NSLOT : NSLOT);
  if (__builtin_expect(type == 0, 1)) {
    v4u d[4], sa, sb;
    const int doff = WS_PART + (tk * 16 + w * 4) * 1024 + lane * 16, soff = WS_STAT + (tk * 4 + w) * 64 + (c & 1) * 32;
    lds_spin_ge(L + F_LAND, 2 * (ne + nkv + 4) - EARLY_GATHER);
#pragma unroll
    for (int k = 0; k < 4; ++k) d[k] = __builtin_amdgcn_raw_buffer_load_b128(rws, doff, k * 1024, AUX_SC1);
    sa = __builtin_amdgcn_raw_buffer_load_b128(rws, soff, 0, AUX_SC1); sb = __builtin_amdgcn_raw_buffer_load_b128(rws, soff + 16, 0, AUX_SC1);
    __builtin_amdgcn_sched_barrier(0);
    lds_spin_ge(L + F_LAND, 2 * (ne + nkv + 4));
    v2u wf[16];
#pragma unroll
    for (int j = 0; j < 4; ++j) {
      const int so = (lslot + j >= NSLOT ? lslot + j - NSLOT : lslot + j) * SLAB + wtro;
#pragma unroll
      for (int k = 0; k < 4; ++k) wf[4 * j + k] = tr_read(L + so + k * 2048);
    }
    lds_t ow = L + L_AH + w * AH_STR + aperm((c & 1) * 128 + (c >> 1) * 16 + r * 4);
    lds_t ap = L + L_AH + p4 * AH_STR + r * 16;
    asm volatile("" : "+v"(ow), "+v"(ap));
    __builtin_amdgcn_sched_barrier(0);
    for (unsigned spin = 0; spin < SPIN_CAP; ++spin) {
      const unsigned long long b0 = BADLANES(d[0]), b1 = BADLANES(d[1]), b2 = BADLANES(d[2]), b3 = BADLANES(d[3]), bs = BADLANES(sa) | BADLANES(sb);
      if (__builtin_expect((b0 | b1 | b2 | b3 | bs) == 0ull, 1)) break;
      __builtin_amdgcn_s_sleep(2);
      asm volatile("" ::: "memory");
      if (b0) d[0] = __builtin_amdgcn_raw_buffer_load_b128(rws, doff, 0, AUX_SC1);
      if (b1) d[1] = __builtin_amdgcn_raw_buffer_load_b128(rws, doff, 1024, AUX_SC1);
      if (b2) d[2] = __builtin_amdgcn_raw_buffer_load_b128(rws, doff, 2048, AUX_SC1);
      if (b3) d[3] = __builtin_amdgcn_raw_buffer_load_b128(rws, doff, 3072, AUX_SC1);
      sa = __builtin_amdgcn_raw_buffer_load_b128(rws, soff, 0, AUX_SC1); sb = __builtin_amdgcn_raw_buffer_load_b128(rws, soff + 16, 0, AUX_SC1);
    }
    const float la[4] = {__uint_as_float(sa.x), __uint_as_float(sa.z), __uint_as_float(sb.x), __uint_as_float(sb.z)};
    const float M = fmaxf(fmaxf(la[0], la[1]), fmaxf(la[2], la[3]));
    float e[4];
#pragma unroll
    for (int k = 0; k < 4; ++k) e[k] = __builtin_amdgcn_exp2f(la[k] - M);
    const float inv = __builtin_amdgcn_rcpf((e[0] + e[1]) + (e[2] + e[3]));
    f32x4 o = e[0] * f32x4{blo(d[0].x), bhi(d[0].x), blo(d[0].z), bhi(d[0].z)};
#pragma unroll
    for (int k = 1; k < 4; ++k) o += e[k] * f32x4{blo(d[k].x), bhi(d[k].x), blo(d[k].z), bhi(d[k].z)};
    o *= inv;
    *(LAS v2u*)ow = v2u{pk2(o[0], o[1]), pk2(o[2], o[3])};
    asm volatile("s_waitcnt lgkmcnt(0)" ::: "memory");
    __builtin_amdgcn_s_barrier();
    asm volatile("" ::: "memory");
    v4u of[8];
#pragma unroll
    for (int j = 0; j < 8; ++j) of[j] = *(LAS v4u*)(ap + j * 64);
    __builtin_amdgcn_sched_barrier(0);
    f32x4 y = {0.f, 0.f, 0.f, 0.f}, y2 = {0.f, 0.f, 0.f, 0.f};
#pragma unroll
    for (int j = 0; j < 8; j += 2) {
      y = mfma32(v4u{wf[2 * j].x, wf[2 * j].y, wf[2 * j + 1].x, wf[2 * j + 1].y}, of[j], y);
      y2 = mfma32(v4u{wf[2 * j + 2].x, wf[2 * j + 2].y, wf[2 * j + 3].x, wf[2 * j + 3].y}, of[j + 1], y2);
    }
    y += y2;
    PUBLISH_F32(WS_SP + tile * 2048, y);
  } else {
    lds_wait_ge(L + F_H, 1);
    f32x4 y = {0.f, 0.f, 0.f, 0.f};
    lds_t ap = L + L_AH + p4 * AH_STR + r * 16;
    int slot = lslot;
#pragma unroll 1
    for (int j = 0; j < 6; ++j) {
      lds_spin_ge(L + F_LAND, 2 * (ne + nkv + j) + 2);
      GEMV_SLAB(slot * SLAB, ap + j * 128, y);
      slot = slot + 1 == NSLOT ? 0 : slot + 1;
    }
    PUBLISH_F32(WS_FP + tile * 2048, y);
  }
#undef PUBLISH_F32
#undef GEMV_SLAB
  if (__builtin_expect(type == 0 && w == 0, 1)) {
    const int p = lane >> 3, lo16 = ob * 512 + (tk & 3) * 128 + (lane & 7) * 16;
    int ooff = lane < 8 ? (ob * DM + ocol) * 2 : 4 * DM * 2;
    asm volatile("" : "+v"(ooff));
    const int o0 = (tcg * 16 + p) * 2048 + lo16, o1 = p < 4 ? o0 + 8 * 2048 : WS_END;
    v4u s0, s1;
    s0 = __builtin_amdgcn_raw_buffer_load_b128(rws, o0, WS_SP, AUX_SC1);
    s1 = __builtin_amdgcn_raw_buffer_load_b128(rws, o0, WS_SP + 8 * 2048, AUX_SC1);
    float ga, gb;
    {
      v4u f0, f1;
      for (unsigned spin = 0; spin < SPIN_CAP; ++spin) {
        asm volatile("" ::: "memory");
        f0 = __builtin_amdgcn_raw_buffer_load_b128(rws, o0, WS_FP, AUX_SC1);
        f1 = __builtin_amdgcn_raw_buffer_load_b128(rws, o1, WS_FP, AUX_SC1);
        if ((BADLANES(f0) | (BADLANES(f1) & 0xffffffffull)) == 0ull) break;
      }
      asm volatile("" ::: "memory");
      if ((BADLANES(s0) | BADLANES(s1)) != 0ull) {
        s0 = __builtin_amdgcn_raw_buffer_load_b128(rws, o0, WS_SP, AUX_SC1);
        s1 = __builtin_amdgcn_raw_buffer_load_b128(rws, o0, WS_SP + 8 * 2048, AUX_SC1);
      }
      __builtin_amdgcn_sched_barrier(0);
      float fa = __uint_as_float(f0.x) + __uint_as_float(f1.x), fb = __uint_as_float(f0.z) + __uint_as_float(f1.z);
      fa += dpp<ROR8>(fa); fb += dpp<ROR8>(fb);
      fa = rbf(xrow_sum(fa)); fb = rbf(xrow_sum(fb));
      ga = rbf(fa * __builtin_amdgcn_rcpf(1.f + __builtin_amdgcn_exp2f(-1.4426950408889634f * fa)));
      gb = rbf(fb * __builtin_amdgcn_rcpf(1.f + __builtin_amdgcn_exp2f(-1.4426950408889634f * fb)));
      asm volatile("" : "+v"(ga), "+v"(gb));
    }
#pragma unroll 1
    for (unsigned spin = 0; spin < SPIN_CAP; ++spin) {
      if ((BADLANES(s0) | BADLANES(s1)) == 0ull) break;
      asm volatile("" ::: "memory");
      s0 = __builtin_amdgcn_raw_buffer_load_b128(rws, o0, WS_SP, AUX_SC1);
      s1 = __builtin_amdgcn_raw_buffer_load_b128(rws, o0, WS_SP + 8 * 2048, AUX_SC1);
    }
    if (__builtin_expect(tile == 0, 0)) __builtin_amdgcn_raw_buffer_store_b32(lds_ld(L + L_EP + (lane & 31) * 4) + 1u, rws, WS_EPOCH + (lane & 31) * 4, 0, AUX_SC1);
    float sa = __uint_as_float(s0.x) + __uint_as_float(s1.x), sb = __uint_as_float(s0.z) + __uint_as_float(s1.z);
    sa += dpp<ROR8>(sa); sb += dpp<ROR8>(sb);
    sa = rbf(xrow_sum(sa)); sb = rbf(xrow_sum(sb));
    const unsigned res = pk2(blo(xres) + rbf(sa * ga), bhi(xres) + rbf(sb * gb));
    __builtin_amdgcn_raw_buffer_store_b32(res, mk_rsrc(out, 4 * DM * 2), ooff, 0, 0);
  }
  return;
  }

  if (w == 5) {
    __builtin_amdgcn_s_setprio(3);
    v4u xv[5]; unsigned ep;
    asm volatile("s_waitcnt vmcnt(0)" : "={v[220:223]}"(xv[0]), "={v[224:227]}"(xv[1]), "={v[228:231]}"(xv[2]), "={v[232:235]}"(xv[3]), "={v[236:239]}"(xv[4]), "={v240}"(ep) :: "memory");
    {
      float s = 0.f;
#pragma unroll
      for (int i = 0; i < 5; ++i)
#pragma unroll
        for (int k = 0; k < 4; ++k) s += blo(xv[i][k]) + bhi(xv[i][k]);
      const float mean = row_sum(s) * (1.f / DM);
      float q = 0.f, q4 = 0.f;
#pragma unroll
      for (int i = 0; i < 5; ++i)
#pragma unroll
        for (int k = 0; k < 4; ++k) {
          const float a = blo(xv[i][k]) - mean, b = bhi(xv[i][k]) - mean;
          if (i < 4) q = fmaf(a, a, fmaf(b, b, q)); else q4 = fmaf(a, a, fmaf(b, b, q4));
        }
      q += c < 8 ? q4 : 0.f;
      const float var = rbf(row_sum(q) * (1.f / DM)), mb = rbf(mean);
      const float inv = __builtin_amdgcn_rcpf(rbf(__builtin_amdgcn_sqrtf(rbf(var + 1e-5f))));
      lds_t ax = L + L_AX + r * AX_STR + (c >> 2) * 64 + (c & 1) * 32 + ((c >> 1) & 1) * 8;
#pragma unroll
      for (int i = 0; i < 5; ++i) {
        unsigned o[4];
#pragma unroll
        for (int k = 0; k < 4; ++k) {
          const unsigned d = pk2(blo(xv[i][k]) - mb, bhi(xv[i][k]) - mb);
          o[k] = pk2(blo(d) * inv, bhi(d) * inv);
        }
        if (i < 4 || c < 8) { *(LAS v2u*)(ax + i * 256) = v2u{o[0], o[1]}; *(LAS v2u*)(ax + i * 256 + 16) = v2u{o[2], o[3]}; }
      }
    }
    unsigned tag = ep;
    tag += dppu<ROR8>(tag); tag += dppu<ROR4>(tag); tag += dppu<ROR2>(tag); tag += dppu<ROR1>(tag);
    tag = (unsigned)__builtin_amdgcn_readlane(tag, 0) + (unsigned)__builtin_amdgcn_readlane(tag, 16) + 0x9E3779B9u;
    tag |= 1u;
    lds_st(L + L_EP + (lane & 31) * 4, ep);
    lds_st(L + F_TAG, tag);
    asm volatile("s_waitcnt lgkmcnt(0)" ::: "memory");
    lds_st(L + F_XN, 1u);
    while (__builtin_amdgcn_s_memrealtime() - t0 < Q_DELAY) __builtin_amdgcn_s_sleep(2);
    {
      int o[4];
      v4u g[4];
#pragma unroll
      for (int k = 0; k < 4; ++k) { o[k] = WS_QP + ((pair & 15) * 4 + r) * 4096 + (k >> 1) * 2048 + (pair >> 4) * 512 + c * 32 + (k & 1) * 16; g[k] = v4u{0u, 0u, 0u, 0u}; }
      for (unsigned spin = 0; spin < SPIN_CAP; ++spin) {
        asm volatile("" ::: "memory");
        v4u a[4];
#pragma unroll
        for (int k = 0; k < 4; ++k) a[k] = __builtin_amdgcn_raw_buffer_load_b128(rws, o[k], 0, AUX_SC1);
        bool done = true;
#pragma unroll
        for (int k = 0; k < 4; ++k) { const bool ok = TAGOK(a[k]); g[k] = ok ? a[k] : g[k]; o[k] = ok ? WS_END : o[k]; done = done & (o[k] == WS_END); }
        if (__all(done)) break;
      }
      *(LAS v2u*)(L + L_Q + lane * 8) = v2u{pk2(__uint_as_float(g[0].x) + __uint_as_float(g[2].x), __uint_as_float(g[0].z) + __uint_as_float(g[2].z)),
                                            pk2(__uint_as_float(g[1].x) + __uint_as_float(g[3].x), __uint_as_float(g[1].z) + __uint_as_float(g[3].z))};
      asm volatile("s_waitcnt lgkmcnt(0)" ::: "memory");
      lds_st(L + F_Q, 1u);
    }
    if (type == 1) {
      const int off = WS_GU + tk * 6 * 1024 + lane * 16;
      lds_t ah = L + L_AH + (c >> 2) * AH_STR + aperm(r * 16 + (c & 3) * 4);
      v4u g[3], u[3], gn[3], un[3];
#pragma unroll
      for (int e = 0; e < 3; ++e) { gn[e] = __builtin_amdgcn_raw_buffer_load_b128(rws, off, e * 1024, AUX_SC1); un[e] = __builtin_amdgcn_raw_buffer_load_b128(rws, off, (72 + e) * 1024, AUX_SC1); }
      unsigned spin = 0;
#pragma unroll 1
      for (int j = 0; j < 6;) {
#pragma unroll
        for (int e = 0; e < 3; ++e) { g[e] = gn[e]; u[e] = un[e]; }
        const bool ok = (BADLANES(g[0]) | BADLANES(u[0]) | BADLANES(g[1]) | BADLANES(u[1]) | BADLANES(g[2]) | BADLANES(u[2])) == 0ull || ++spin >= SPIN_CAP;
        const int jn = ok ? j + 3 : j;
        asm volatile("" ::: "memory");
        if (jn < 6) {
#pragma unroll
          for (int e = 0; e < 3; ++e) { gn[e] = __builtin_amdgcn_raw_buffer_load_b128(rws, off + jn * 1024, e * 1024, AUX_SC1); un[e] = __builtin_amdgcn_raw_buffer_load_b128(rws, off + jn * 1024, (72 + e) * 1024, AUX_SC1); }
        }
        if (ok) {
#pragma unroll
          for (int e = 0; e < 3; ++e) {
            const float gv[4] = {blo(g[e].x), bhi(g[e].x), blo(g[e].z), bhi(g[e].z)}, uv[4] = {blo(u[e].x), bhi(u[e].x), blo(u[e].z), bhi(u[e].z)};
            float hv[4];
#pragma unroll
            for (int i = 0; i < 4; ++i) {
              const float a = gv[i], inner = 0.7978845608028654f * fmaf(0.044715f * a * a, a, a);
              hv[i] = rbf(a * __builtin_amdgcn_rcpf(1.f + __builtin_amdgcn_exp2f(-2.8853900817779268f * inner))) * uv[i];
            }
            *(LAS v2u*)(ah + (j + e) * 128) = v2u{pk2(hv[0], hv[1]), pk2(hv[2], hv[3])};
          }
        }
        j = jn;
      }
      asm volatile("s_waitcnt lgkmcnt(0)" ::: "memory");
      lds_st(L + F_H, 1u);
    }
    return;
  }

  {
    __builtin_amdgcn_s_setprio(3);
    {
    const int stride = nq ? QD * 2 : FF * 2;
    const rsrc_t rw = mk_rsrc(nq ? wq + (qj >> 1) * 64 : gubase, 575 * stride + 128);
    const int voff = prow * stride + pch * 16, so0 = nq ? qs0 * 64 * stride : 0;
#pragma unroll 1
    for (int s = 0; s < n0; ++s) dma_wslab(rw, voff, so0 + s * 64 * stride, 8 * stride, ring + s * SLAB);
    }
    int s = n0, pub = 0;
#define PUB(v) do { pub = max(pub, (v)); lds_st(L + F_LAND, pub); } while (0)
    {
      const rsrc_t rw = mk_rsrc(gubase, gj < 0 ? 0 : 575 * (FF * 2) + 128);
      const int voff = prow * (FF * 2) + pch * 16;
#pragma unroll 1
      for (; s < ne; ++s) {
        wait_vm<8 * (WS_E - 1)>();
        PUB(2 * (s - (WS_E - 1)));
        dma_wslab(rw, voff, (s - nq) * 64 * (FF * 2), 8 * (FF * 2), ring + s * SLAB);
      }
    }
    int slot = ne;
#define RINGSPACE(last) do { \
      if ((last) - NSLOT >= freed) { \
        bool drained = false; \
        for (;;) { \
          asm volatile("" ::: "memory"); \
          const v4u cv = *(volatile LAS v4u*)(L + F_CONS); \
          freed = __builtin_amdgcn_readfirstlane(min(min(cv.x, cv.y), min(cv.z, cv.w))); \
          if ((last) - NSLOT < freed) break; \
          if (!drained) { wait_vm<0>(); PUB(2 * s); drained = true; } \
          __builtin_amdgcn_s_sleep(1); \
        } \
      } } while (0)
    int freed = 0;
    {
      const size_t kvbase = ((size_t)pair * SK + (SK - WIN) + ((jx >> 6) & 127) * 16) * DH;
      const rsrc_t rk = mk_rsrc(Kc + kvbase, nkv * 4096), rv = mk_rsrc(Vc + kvbase, nkv * 4096);
      const unsigned tk0 = (unsigned)__builtin_amdgcn_s_memrealtime();
      const int dt64 = ((type == 1 ? T_NOM1 : T_NOM) - (int)(tk0 - (unsigned)t0)) * 64 / nkv;
      int vk[4], vv[4];
#pragma unroll
      for (int p = 0; p < 4; ++p) { vk[p] = p * 1024 + r * 256 + ((c ^ (4 * p + r)) << 4); vv[p] = p * 1024 + r * 256 + ((c ^ (((4 * p + r) & 7) << 1)) << 4); }
      unsigned now = tk0;
      bool wide = false;
#pragma unroll 1
      for (int i = 0; i < nkv; ++i, ++s) {
        const int lag = (int)(now - tk0) * 64 - (i - 1) * dt64;
        const bool boost = lag > LAG_HI * 64 && i >= BOOST_FROM;
        if (boost || wide) { wait_vm<44>(); PUB(2 * s - 11); wait_vm<40>(); PUB(2 * s - 10); }
        if (!boost) {
          if (wide) { wait_vm<36>(); PUB(2 * s - 9); wait_vm<32>(); PUB(2 * s - 8); }
          wait_vm<28>(); PUB(2 * s - 7); wait_vm<24>(); PUB(2 * s - 6);
          if (lag < -LEAD_MAX * 64) {
            wait_vm<20>(); PUB(2 * s - 5); wait_vm<16>(); PUB(2 * s - 4);
            while ((int)((unsigned)__builtin_amdgcn_s_memrealtime() - tk0) * 64 - i * dt64 < -LEAD_MAX * 64) __builtin_amdgcn_s_sleep(1);
          }
        }
        wide = boost;
        RINGSPACE(s);
        now = (unsigned)__builtin_amdgcn_s_memrealtime();
        dma_kvslab(rk, rv, vk, vv, i * 4096, ring + slot * SLAB);
        slot = slot + 1 == NSLOT ? 0 : slot + 1;
      }
    }
    const rsrc_t rl = mk_rsrc(type == 0 ? wo + tk * 256 * DM + tcg * 64 : wdn + tk * 384 * DM + tcg * 64, (nl * 64 - 1) * (DM * 2) + 128);
    const int lvoff = prow * (DM * 2) + pch * 16;
    if (nl) RINGSPACE(s + nl - 1);
    const bool behind = (int)((unsigned)__builtin_amdgcn_s_memrealtime() - (unsigned)t0) > (type == 1 ? T_NOM1 + LATE_THR : T_NOM + LATE_THR);
    wait_vm<44>(); PUB(2 * s - 11); wait_vm<40>(); PUB(2 * s - 10); wait_vm<36>(); PUB(2 * s - 9); wait_vm<32>(); PUB(2 * s - 8);
    wait_vm<28>(); PUB(2 * s - 7); wait_vm<24>(); PUB(2 * s - 6); wait_vm<20>(); PUB(2 * s - 5); wait_vm<16>(); PUB(2 * s - 4);
    if (!behind) { wait_vm<12>(); PUB(2 * s - 3); wait_vm<8>(); PUB(2 * s - 2); wait_vm<4>(); PUB(2 * s - 1); wait_vm<0>(); PUB(2 * s); }
#pragma unroll 1
    for (int i = 0; i < nl; ++i, ++s) {
      if (behind) {
        if (i < 2) { wait_vm<12>(); PUB(2 * (s - 2) + 1); wait_vm<8>(); PUB(2 * (s - 1)); }
      } else if (i == LATE_FIRST) {
        lds_spin_ge(L + F_PART, 5);
      }
      dma_wslab(rl, lvoff, i * 64 * (DM * 2), 8 * (DM * 2), ring + slot * SLAB);
      slot = slot + 1 == NSLOT ? 0 : slot + 1;
    }
    wait_vm<40>(); PUB(2 * s - 10); wait_vm<32>(); PUB(2 * s - 8); wait_vm<24>(); PUB(2 * s - 6);
    wait_vm<16>(); PUB(2 * s - 4); wait_vm<8>(); PUB(2 * s - 2); wait_vm<4>(); PUB(2 * s - 1); wait_vm<0>(); PUB(2 * s);
#undef RINGSPACE
#undef PUB
  }
}

constexpr int PEN3 = 70;
constexpr int LATE_COST = 10;
constexpr int ODDPEN = 5;
static void build_jobs(Jobs& J) {
  int wgs[3][256], nw[3] = {0, 0, 0};
  int type[256], tile[256], qj[256], gj[256], cost[256];
  for (int wg = 0; wg < 256; ++wg) {
    const int xcd = wg & 7, idx = wg >> 3, gi = idx & 15, o = (idx >> 4) * 7 + gi - 9;
    const int j = (xcd & 1) * 5 + o - 9;
    type[wg] = gi < 9 ? 0 : (o < 9 || j < 9) ? 1 : 2;
    tile[wg] = gi < 9 ? gi * 16 + xcd * 2 + (idx >> 4) : o < 9 ? o * 16 + xcd : j < 9 ? j * 16 + 8 + (xcd >> 1) : 0;
    wgs[type[wg]][nw[type[wg]]++] = wg;
    qj[wg] = gj[wg] = -1;
  }
  int fast[126], nf = 0, slow[18], nsl = 0, halves[128], nh = 0;
  for (int g = 0; g < 144; ++g) { if ((g & 7) == 3) slow[nsl++] = g; else fast[nf++] = g; }
  for (int h = 0; h < 128; ++h) if (((h >> 1) & 7) == 3) halves[nh++] = h;
  for (int h = 0; h < 128; ++h) if (((h >> 1) & 7) != 3 && !(h & 1)) halves[nh++] = h;
  for (int h = 0; h < 128; ++h) if (((h >> 1) & 7) != 3 && (h & 1)) halves[nh++] = h;
  int ih = 0;
  for (int i = 0; i < 4; ++i) gj[wgs[2][i]] = slow[i];
  for (int i = 0; i < 108; ++i) { if (i < 94) qj[wgs[1][i]] = halves[ih++]; else gj[wgs[1][i]] = slow[i - 90]; }
  for (int i = 0; i < 144; ++i) { if (i < 34) qj[wgs[0][i]] = halves[ih++]; if (i >= 18) gj[wgs[0][i]] = fast[i - 18]; }
  for (int wg = 0; wg < 256; ++wg) {
    const int nq = qj[wg] < 0 ? 0 : 5 - (qj[wg] & 1);
    cost[wg] = 10 * nq + (nq && ((qj[wg] >> 1) & 7) == 3 ? PEN3 * nq / 9 : 0) + (gj[wg] < 0 ? 0 : (gj[wg] & 7) == 3 ? 90 + PEN3 : 90) +
               (type[wg] == 1 ? LATE_COST : 0) + ((wg & 1) ? ODDPEN : 0);
  }
  int ord[256];
  for (int i = 0; i < 256; ++i) ord[i] = i;
  for (int i = 1; i < 256; ++i) { const int v = ord[i]; int j = i; while (j > 0 && cost[ord[j - 1]] < cost[v]) { ord[j] = ord[j - 1]; --j; } ord[j] = v; }
  int grp[64][4], sum4[64];
  for (int p = 0; p < 64; ++p) {
    grp[p][0] = ord[p]; grp[p][1] = ord[127 - p]; grp[p][2] = ord[128 + p]; grp[p][3] = ord[255 - p];
    sum4[p] = cost[grp[p][0]] + cost[grp[p][1]] + cost[grp[p][2]] + cost[grp[p][3]];
  }
  for (int pass = 0; pass < 3; ++pass)
    for (int p = 0; p < 64; ++p) for (int q = 0; q < 64; ++q) for (int a = 0; a < 4; ++a) for (int b = 0; b < 4; ++b) {
      const int d = cost[grp[p][a]] - cost[grp[q][b]], gap = sum4[p] - sum4[q];
      if (d > 0 && d < gap) { const int t = grp[p][a]; grp[p][a] = grp[q][b]; grp[q][b] = t; sum4[p] -= d; sum4[q] += d; }
    }
  for (int p = 0; p < 64; ++p) {
    const int* mem = grp[p];
    const int T = (1280 + sum4[p]) / 4;
    int n[4], sum = 0;
    for (int j = 0; j < 4; ++j) { n[j] = (T - cost[mem[j]] + 5) / 10; sum += n[j]; }
    for (int j = 3; sum != 128; j = (j + 3) & 3) { if (sum < 128) { ++n[j]; ++sum; } else { --n[3 - j]; --sum; } }
    int s0 = 0;
    for (int j = 0; j < 4; ++j) {
      const int wg = mem[j];
      J.v[2 * wg] = (unsigned)p | (unsigned)s0 << 6 | (unsigned)n[j] << 13 | (unsigned)j << 19 | (unsigned)type[wg] << 21;
      J.v[2 * wg + 1] = (unsigned)(qj[wg] + 1) | (unsigned)(gj[wg] + 1) << 8 | (unsigned)tile[wg] << 16;
      s0 += n[j];
    }
  }
}
}

extern "C" void kernel_launch(void* const* d_in, const int* in_sizes, int n_in, void* d_out, int out_size, void* d_ws, size_t ws_size, hipStream_t stream) {
  Jobs J;
  build_jobs(J);
  fused_decode<<<256, 384, 0, stream>>>((const u16*)d_in[0], (const u16*)d_in[1], (const u16*)d_in[2], (const u16*)d_in[3], (const u16*)d_in[4],
                                        (const u16*)d_in[6], (const u16*)d_in[7], (const u16*)d_in[8], (u16*)d_out, (unsigned char*)d_ws, J);
}
```
